```python
import torch

def _softmax(x, axis=-1):
    return torch.softmax(x, dim=axis)
def _gelu(x):
    return torch.nn.functional.gelu(x, approximate="tanh")
def _silu(x):
    return torch.nn.functional.silu(x)
def _sigmoid(x):
    return torch.sigmoid(x)
def _layernorm(x, axis=-1, eps=1e-5):
    m = x.mean(dim=axis, keepdim=True)
    v = x.var(dim=axis, keepdim=True, unbiased=False)
    return (x - m) / torch.sqrt(v + eps)
def _scatter_add(tgt, idx, src, axis=0):
    return tgt.clone().index_add_(axis, idx, src)
def _segment_sum(src, seg, n):
    out = torch.zeros((n,) + src.shape[1:], dtype=src.dtype)
    return out.index_add_(0, seg, src)
def _pad(x, pw):
    flat = tuple(v for pair in reversed(pw) for v in pair)
    return torch.nn.functional.pad(x, flat)

kernel_name = 'recurrent_scan__sliding_block_cached__dense_activation_gelu__decode'

def setup_inputs():

    _w = (lambda a: (lambda t: t.to(torch.bfloat16) if t.is_floating_point() else t)(torch.as_tensor(a)))
    def _A_setup():
        B, Sq, D = 8, 1, 512
        Hq, Hkv, Sk, Dh = 32, 8, 4096, 128
        assert Hq % Hkv == 0, (Hq, Hkv)
        return {
            "x": _w((lambda *s: torch.randn(*s, dtype=torch.bfloat16) * 0.05)(B, Sq, D)),
            "k_cache": _w((lambda *s: torch.randn(*s, dtype=torch.bfloat16) * 3.2)(B, Hkv, Sk, Dh)),
            "v_cache": _w((lambda *s: torch.randn(*s, dtype=torch.bfloat16) * 3.2)(B, Hkv, Sk, Dh)),
            "wq": _w((lambda *s: torch.randn(*s, dtype=torch.bfloat16) * 3.2)(D, Hq * Dh) * (D ** -0.5)),
            "wo": _w((lambda *s: torch.randn(*s, dtype=torch.bfloat16) * 3.2)(Hq * Dh, D) * ((Hq * Dh) ** -0.5)),
        }
    _A_in = _A_setup()

    def _B_setup():
        B, S, D, F = 8, 1, 512, 1792
        return {
            "x": _w((lambda *s: torch.randn(*s, dtype=torch.bfloat16) * 0.05)(B, S, D)),
            "w1": _w((lambda *s: torch.randn(*s, dtype=torch.bfloat16) * 3.2)(D, F) * (D ** -0.5)),
            "b1": _w((lambda *s: torch.randn(*s, dtype=torch.bfloat16) * 3.2)(F) * 0.01),
            "w2": _w((lambda *s: torch.randn(*s, dtype=torch.bfloat16) * 0.4)(F, D) * (F ** -0.5)),
            "b2": _w((lambda *s: torch.randn(*s, dtype=torch.bfloat16) * 0.4)(D) * 0.01),
        }
    _B_in = _B_setup()

    return {
        "A_x": _A_in["x"],
        "A_k_cache": _A_in["k_cache"],
        "A_v_cache": _A_in["v_cache"],
        "A_wq": _A_in["wq"],
        "A_wo": _A_in["wo"],
        "B_x": _B_in["x"],
        "B_w1": _B_in["w1"],
        "B_b1": _B_in["b1"],
        "B_w2": _B_in["w2"],
        "B_b2": _B_in["b2"],
    }

def reference(A_x, A_k_cache, A_v_cache, A_wq, A_wo, B_x, B_w1, B_b1, B_w2, B_b2):
    def _A_ref(x, k_cache, v_cache, wq, wo):
        B, Sq, D = x.shape
        Hq, Dh = 32, 128
        Hkv, Sk = k_cache.shape[1], k_cache.shape[2]
        W = min(4096, max(256, Sk // 4))
        rep = Hq // Hkv
        q = (lambda x, *d: x.permute(*d))((x @ wq).reshape(B, Sq, Hq, Dh), 0, 2, 1, 3)
        k = (lambda x, n, axis: torch.repeat_interleave(x, n, dim=axis))(k_cache[:, :, Sk - W:, :], rep, axis=1)
        v = (lambda x, n, axis: torch.repeat_interleave(x, n, dim=axis))(v_cache[:, :, Sk - W:, :], rep, axis=1)
        scores = torch.einsum("bhqd,bhkd->bhqk", q, k) * (Dh ** -0.5)
        p = _softmax(scores, axis=-1)
        o = torch.einsum("bhqk,bhkd->bhqd", p, v)
        return (lambda x, *d: x.permute(*d))(o, 0, 2, 1, 3).reshape(B, Sq, Hq * Dh) @ wo

    def _B_ref(x, w1, b1, w2, b2):
        h = torch.einsum("bsd,df->bsf", x, w1) + b1
        h = _gelu(h)
        return torch.einsum("bsf,fd->bsd", h, w2) + b2

    x = A_x
    xn = _layernorm(x, axis=-1)
    s = _A_ref(xn, A_k_cache, A_v_cache, A_wq, A_wo)
    g = _silu(_B_ref(xn, B_w1, B_b1, B_w2, B_b2))
    return x + s * g
```

```cpp
#include <hip/hip_runtime.h>
#include <hip/hip_bf16.h>
#include <stdint.h>

typedef unsigned short u16;
__device__ __forceinline__ float bf2f(u16 v) { return __uint_as_float(((unsigned)v) << 16); }
__device__ __forceinline__ float rbf(float f) {
  unsigned u = __float_as_uint(f);
  u = (u + 0x7FFFu + ((u >> 16) & 1u)) & 0xFFFF0000u;
  return __uint_as_float(u);
}
__device__ __forceinline__ u16 f2bf(float f) { return (u16)(__float_as_uint(rbf(f)) >> 16); }

constexpr int B = 8, D = 512, HQ = 32, HKV = 8, SK = 4096, DH = 128, W = 1024, F = 1792;

__device__ void compute_xn(const u16* x, float* xn  , float* red) {
  int tid = threadIdx.x;
  int row = tid >> 5, l = tid & 31;
  float v[16]; float s = 0.f;
  for (int i = 0; i < 16; ++i) { v[i] = bf2f(x[row * D + l + 32 * i]); s += v[i]; }
  for (int o = 16; o >= 1; o >>= 1) s += __shfl_xor(s, o);
  float mean = s * (1.f / D);
  float q = 0.f;
  for (int i = 0; i < 16; ++i) { float d = v[i] - mean; q += d * d; }
  for (int o = 16; o >= 1; o >>= 1) q += __shfl_xor(q, o);
  float var = rbf(q * (1.f / D));
  float mb = rbf(mean);
  float den = rbf(sqrtf(rbf(var + 1e-5f)));
  for (int i = 0; i < 16; ++i) xn[row * D + l + 32 * i] = rbf(rbf(v[i] - mb) / den);
  __syncthreads();
}

__global__ void k1(const u16* x, const u16* wq, const u16* w1, const u16* b1, float* q, float* h) {
  __shared__ float xn[B * D];
  compute_xn(x, xn, nullptr);
  int col = blockIdx.x * 256 + threadIdx.x;
  float acc[8] = {0, 0, 0, 0, 0, 0, 0, 0};
  if (col < HQ * DH) {
    for (int k = 0; k < D; ++k) { float w = bf2f(wq[(size_t)k * (HQ * DH) + col]); for (int b = 0; b < 8; ++b) acc[b] += xn[b * D + k] * w; }
    for (int b = 0; b < 8; ++b) q[b * HQ * DH + col] = rbf(acc[b]);
  } else {
    int c = col - HQ * DH;
    for (int k = 0; k < D; ++k) { float w = bf2f(w1[(size_t)k * F + c]); for (int b = 0; b < 8; ++b) acc[b] += xn[b * D + k] * w; }
    for (int b = 0; b < 8; ++b) {
      float t = rbf(rbf(acc[b]) + bf2f(b1[c]));
      float u = 0.7978845608028654f * (t + 0.044715f * t * t * t);
      h[b * F + c] = rbf(0.5f * t * (1.f + tanhf(u)));
    }
  }
}

__global__ void k2(const u16* kc, const u16* vc, const float* q, float* o) {
  __shared__ float sc[W]; __shared__ float qs[DH]; __shared__ float red[256];
  int b = blockIdx.x / HQ, hh = blockIdx.x % HQ, kvh = hh / 4, tid = threadIdx.x;
  if (tid < DH) qs[tid] = q[b * HQ * DH + hh * DH + tid];
  __syncthreads();
  const u16* kb = kc + ((size_t)(b * HKV + kvh) * SK + (SK - W)) * DH;
  const u16* vb = vc + ((size_t)(b * HKV + kvh) * SK + (SK - W)) * DH;
  float mx = -INFINITY;
  for (int k = tid; k < W; k += 256) {
    float a = 0.f;
    for (int d = 0; d < DH; ++d) a += qs[d] * bf2f(kb[(size_t)k * DH + d]);
    a = rbf(rbf(a) * 0.08838834764831845f);
    sc[k] = a; mx = fmaxf(mx, a);
  }
  red[tid] = mx; __syncthreads();
  for (int s = 128; s >= 1; s >>= 1) { if (tid < s) red[tid] = fmaxf(red[tid], red[tid + s]); __syncthreads(); }
  mx = red[0]; __syncthreads();
  float sum = 0.f;
  for (int k = tid; k < W; k += 256) { float e = expf(sc[k] - mx); sc[k] = e; sum += e; }
  red[tid] = sum; __syncthreads();
  for (int s = 128; s >= 1; s >>= 1) { if (tid < s) red[tid] += red[tid + s]; __syncthreads(); }
  sum = red[0];
  for (int k = tid; k < W; k += 256) sc[k] = rbf(sc[k] / sum);
  __syncthreads();
  if (tid < DH) {
    float a = 0.f;
    for (int k = 0; k < W; ++k) a += sc[k] * bf2f(vb[(size_t)k * DH + tid]);
    o[b * HQ * DH + hh * DH + tid] = rbf(a);
  }
}

__global__ void k3(const u16* x, const u16* wo, const u16* w2, const u16* b2, const float* o, const float* h, u16* out) {
  int b = blockIdx.x / 2, c = (blockIdx.x % 2) * 256 + threadIdx.x;
  float s = 0.f;
  for (int k = 0; k < HQ * DH; ++k) s += o[b * HQ * DH + k] * bf2f(wo[(size_t)k * D + c]);
  s = rbf(s);
  float y = 0.f;
  for (int k = 0; k < F; ++k) y += h[b * F + k] * bf2f(w2[(size_t)k * D + c]);
  y = rbf(rbf(y) + bf2f(b2[c]));
  float g = rbf(y / (1.f + expf(-y)));
  out[b * D + c] = f2bf(bf2f(x[b * D + c]) + rbf(s * g));
}

extern "C" void kernel_launch(void* const* d_in, const int* in_sizes, int n_in,
                              void* d_out, int out_size, void* d_ws, size_t ws_size,
                              hipStream_t stream) {
  const u16* x = (const u16*)d_in[0]; const u16* kc = (const u16*)d_in[1]; const u16* vc = (const u16*)d_in[2];
  const u16* wq = (const u16*)d_in[3]; const u16* wo = (const u16*)d_in[4];
  const u16* w1 = (const u16*)d_in[6]; const u16* b1 = (const u16*)d_in[7]; const u16* w2 = (const u16*)d_in[8]; const u16* b2 = (const u16*)d_in[9];
  float* q = (float*)d_ws; float* h = q + B * HQ * DH; float* o = h + B * F;
  k1<<<(HQ * DH + F) / 256, 256, 0, stream>>>(x, wq, w1, b1, q, h);
  k2<<<B * HQ, 256, 0, stream>>>(kc, vc, q, o);
  k3<<<B * 2, 256, 0, stream>>>(x, wo, w2, b2, o, h, (u16*)d_out);
}
```

```cpp
#include <hip/hip_runtime.h>
#include <stdint.h>

typedef unsigned short u16;
typedef unsigned u32;
typedef __attribute__((ext_vector_type(4))) unsigned u32x4;
typedef __attribute__((ext_vector_type(2))) unsigned u32x2;
typedef __attribute__((ext_vector_type(2))) float f32x2;
typedef __attribute__((ext_vector_type(4))) float f32x4;
typedef __attribute__((ext_vector_type(8))) __bf16 bf16x8;
typedef __attribute__((ext_vector_type(2))) __bf16 bf16x2;
typedef __attribute__((ext_vector_type(4))) short s16x4;
#define LAS __attribute__((address_space(3)))

#define KPRE 4

namespace {

constexpr int NB = 8, DM = 512, HQ = 32, HKV = 8, SK = 4096, DH = 128, WIN = 1024, FF = 1792;
constexpr u32 MB = 1u << 20;
constexpr u32 QP_OFF = 4096, HP_OFF = QP_OFF + MB, OP_OFF = HP_OFF + MB, OS_OFF = OP_OFF + MB,
              SP_OFF = OS_OFF + 65536, YP_OFF = SP_OFF + 2 * MB, WS_END = YP_OFF + 2 * MB;
constexpr u32 OOB = 0x7F000000u;
constexpr int SPIN_CAP = 400000;

#define LD_NT(rs, off) __builtin_amdgcn_raw_buffer_load_b128(rs, off, 0, 2)
#define LD_SC(rs, off) __builtin_amdgcn_raw_buffer_load_b128(rs, off, 0, 16)
#define ST_SC(v, rs, off) __builtin_amdgcn_raw_buffer_store_b128(v, rs, off, 0, 16)
#define PIN() asm volatile("" ::: "memory")

__device__ __forceinline__ float bf_lo(u32 v) { return __uint_as_float(v << 16); }
__device__ __forceinline__ float bf_hi(u32 v) { return __uint_as_float(v & 0xFFFF0000u); }
__device__ __forceinline__ float rbf(float f) { return (float)(__bf16)f; }
__device__ __forceinline__ u32 pack_bf(float lo, float hi) {
  f32x2 v = {lo, hi};
  return __builtin_bit_cast(u32, __builtin_convertvector(v, bf16x2));
}
__device__ __forceinline__ float asf(u32 v) { return __uint_as_float(v); }
__device__ __forceinline__ u32 asu(float v) { return __float_as_uint(v); }
template <int CTRL>
__device__ __forceinline__ float dpp(float v) {
  return __int_as_float(__builtin_amdgcn_update_dpp(0, __float_as_int(v), CTRL, 0xF, 0xF, true));
}
__device__ __forceinline__ float xor16_add(float v) {
  auto r = __builtin_amdgcn_permlane16_swap(asu(v), asu(v), false, false);
  return asf(r[0]) + asf(r[1]);
}
__device__ __forceinline__ float xor32_add(float v) {
  auto r = __builtin_amdgcn_permlane32_swap(asu(v), asu(v), false, false);
  return asf(r[0]) + asf(r[1]);
}
__device__ __forceinline__ float xor16_max(float v) {
  auto r = __builtin_amdgcn_permlane16_swap(asu(v), asu(v), false, false);
  return fmaxf(asf(r[0]), asf(r[1]));
}
__device__ __forceinline__ float xor32_max(float v) {
  auto r = __builtin_amdgcn_permlane32_swap(asu(v), asu(v), false, false);
  return fmaxf(asf(r[0]), asf(r[1]));
}
__device__ __forceinline__ float wave_sum(float v) {
  v += dpp<0xB1>(v);
  v += dpp<0x4E>(v);
  v += dpp<0x141>(v);
  v += dpp<0x140>(v);
  return xor32_add(xor16_add(v));
}
__device__ __forceinline__ bf16x8 as_bf(u32x4 v) { return __builtin_bit_cast(bf16x8, v); }
__device__ __forceinline__ f32x4 mfma(u32x4 a, u32x4 b, f32x4 c) {
  return __builtin_amdgcn_mfma_f32_16x16x32_bf16(as_bf(a), as_bf(b), c, 0, 0, 0);
}
__device__ __forceinline__ u32x4 tr_col(const u32x4* R, int e) {
  const int d = e >> 1;
  const u32 sel = (e & 1) ? 0x07060302u : 0x05040100u;
  u32x4 o;
  o[0] = __builtin_amdgcn_perm(R[1][d], R[0][d], sel);
  o[1] = __builtin_amdgcn_perm(R[3][d], R[2][d], sel);
  o[2] = __builtin_amdgcn_perm(R[5][d], R[4][d], sel);
  o[3] = __builtin_amdgcn_perm(R[7][d], R[6][d], sel);
  return o;
}
__device__ __forceinline__ u32x4 tr_read(const char* p, int stride) {
  s16x4 a = __builtin_amdgcn_ds_read_tr16_b64_v4i16((LAS s16x4*)p);
  s16x4 b = __builtin_amdgcn_ds_read_tr16_b64_v4i16((LAS s16x4*)(p + 4 * stride));
  u32x2 ua = __builtin_bit_cast(u32x2, a), ub = __builtin_bit_cast(u32x2, b);
  return u32x4{ua[0], ua[1], ub[0], ub[1]};
}
__device__ __forceinline__ bool tag_ok(const u32x4& v, u32 tag) { return (v[1] == tag) & (v[3] == tag); }
__device__ __forceinline__ u32x4 unit_of(const f32x4& D, bool lo, u32 tag) {
  return u32x4{asu(lo ? D[0] : D[2]), tag, asu(lo ? D[1] : D[3]), tag};
}

#define STAMP(i) do { } while (0)

constexpr int XN_LD = 520;
constexpr int OA_LD = 72;
constexpr int HA_LD = 40;
constexpr int S64 = 160;
constexpr int S128 = 288;

__global__ void __launch_bounds__(512, 2)
fused_kernel(const u16* __restrict__ x, const u16* __restrict__ kc, const u16* __restrict__ vc,
             const u16* __restrict__ wq, const u16* __restrict__ wo, const u16* __restrict__ w1,
             const u16* __restrict__ b1, const u16* __restrict__ w2, const u16* __restrict__ b2,
             u16* out, char* ws) {
  __shared__ __attribute__((aligned(16))) char s_wa[192 * S64];
  __shared__ __attribute__((aligned(16))) char s_wb[96 * S128];
  __shared__ __attribute__((aligned(16))) u16 s_xn[8 * XN_LD];
  __shared__ __attribute__((aligned(16))) u16 s_q[512];
  __shared__ __attribute__((aligned(16))) u16 s_oA[8 * OA_LD];
  __shared__ __attribute__((aligned(16))) u16 s_hA[8 * HA_LD];
  __shared__ __attribute__((aligned(16))) float s_m[8 * 4];
  __shared__ __attribute__((aligned(16))) float s_l[8 * 4];
  __shared__ __attribute__((aligned(16))) float s_o[8 * 512];
  __shared__ __attribute__((aligned(16))) float s_red[8 * 32];

  const int bid = blockIdx.x, tid = threadIdx.x, lane = tid & 63;
  const int wave = __builtin_amdgcn_readfirstlane(tid >> 6);
  const int kvh = bid & 7, r = bid >> 3;
  const int ab = r >> 2, aj = r & 3;
  const bool ffn = bid < 224;
  const int c28 = bid >> 3, fidx = bid & 7;
  const u32 foob = ffn ? 0u : OOB;
  const int n = lane & 15, g = lane >> 4;
  const bool lo = n < 8;
  STAMP(0);

  auto rs_x = __builtin_amdgcn_make_buffer_rsrc((void*)x, 0, NB * DM * 2, 0x00020000);
  auto rs_k = __builtin_amdgcn_make_buffer_rsrc((void*)kc, 0, NB * HKV * SK * DH * 2, 0x00020000);
  auto rs_v = __builtin_amdgcn_make_buffer_rsrc((void*)vc, 0, NB * HKV * SK * DH * 2, 0x00020000);
  auto rs_wq = __builtin_amdgcn_make_buffer_rsrc((void*)wq, 0, DM * HQ * DH * 2, 0x00020000);
  auto rs_wo = __builtin_amdgcn_make_buffer_rsrc((void*)wo, 0, DM * HQ * DH * 2, 0x00020000);
  auto rs_w1 = __builtin_amdgcn_make_buffer_rsrc((void*)w1, 0, DM * FF * 2, 0x00020000);
  auto rs_w2 = __builtin_amdgcn_make_buffer_rsrc((void*)w2, 0, DM * FF * 2, 0x00020000);
  auto rs_b1 = __builtin_amdgcn_make_buffer_rsrc((void*)b1, 0, FF * 2, 0x00020000);
  auto rs_b2 = __builtin_amdgcn_make_buffer_rsrc((void*)b2, 0, DM * 2, 0x00020000);
  auto rs_w = __builtin_amdgcn_make_buffer_rsrc((void*)ws, 0, WS_END, 0x00020000);

  u32x4 xv = __builtin_amdgcn_raw_buffer_load_b128(rs_x, (u32)(wave * DM + lane * 8) * 2u, 0, 0);
  u32 epoch = __builtin_amdgcn_raw_buffer_load_b32(rs_w, 0, 0, 16);
  u32x4 Wt[3];
  {
    const u32 o = (u32)((128 * aj + 16 * wave + (lane >> 3)) * (HQ * DH) + kvh * 512 + 64 * ab + 8 * (lane & 7)) * 2u;
    Wt[0] = LD_NT(rs_wq, o);
    Wt[1] = LD_NT(rs_wq, o + (u32)(8 * HQ * DH) * 2u);
    Wt[2] = LD_NT(rs_w1, foob + (u32)((64 * fidx + 8 * wave + (lane >> 3)) * FF + 64 * c28 + 8 * (lane & 7)) * 2u);
  }
  PIN();
  const u32 kvbase = (u32)(((ab * HKV + kvh) * SK + (SK - WIN) + aj * 256 + wave * 32) * DH) * 2u;
  const u32 koff = kvbase + (u32)((n & 7) * 256 + (n >> 3) * 64 + g * 16);
  u32x4 Kr[8];
#pragma unroll
  for (int i = 0; i < KPRE; ++i) Kr[i] = LD_NT(rs_k, koff + (u32)((i >> 1) * 2048 + (i & 1) * 128));
  PIN();

  {
    float v[8];
#pragma unroll
    for (int i = 0; i < 4; ++i) { v[2 * i] = bf_lo(xv[i]); v[2 * i + 1] = bf_hi(xv[i]); }
    float s = ((v[0] + v[1]) + (v[2] + v[3])) + ((v[4] + v[5]) + (v[6] + v[7]));
    float mean = wave_sum(s) * (1.f / DM);
    float q = 0.f;
#pragma unroll
    for (int i = 0; i < 8; ++i) { float d = v[i] - mean; q += d * d; }
    float var = rbf(wave_sum(q) * (1.f / DM));
    float mb = rbf(mean);
    float den = rbf(sqrtf(rbf(var + 1e-5f)));
    float inv = 1.f / den;
    u32x4 o;
#pragma unroll
    for (int i = 0; i < 4; ++i) {
      u32 d = pack_bf(v[2 * i] - mb, v[2 * i + 1] - mb);
      o[i] = pack_bf(bf_lo(d) * inv, bf_hi(d) * inv);
    }
    *(u32x4*)&s_xn[wave * XN_LD + lane * 8] = o;
  }
  const u32 tag = __builtin_amdgcn_readfirstlane(epoch) + 0x9E3779B9u;
  {
    char* p = s_wa + (16 * wave + (lane >> 3)) * S64 + (lane & 7) * 16;
    *(u32x4*)p = Wt[0];
    *(u32x4*)(p + 8 * S64) = Wt[1];
    *(u32x4*)(s_wa + (128 + 8 * wave + (lane >> 3)) * S64 + (lane & 7) * 16) = Wt[2];
  }
  __syncthreads();
  STAMP(1);

  const int trl64 = (8 * g + (n >> 2)) * S64 + (n & 3) * 8;
  const int trl128 = (8 * g + (n >> 2)) * S128 + (n & 3) * 8;
  {
    const bool isq = wave < 4;
    const int cb = wave & 3;
    const char* wp = s_wa + (isq ? 0 : 128 * S64) + cb * 32 + trl64;
    const u16* xp = &s_xn[(n & 7) * XN_LD + (isq ? 128 * aj : 64 * fidx) + 8 * g];
    f32x4 D = {0.f, 0.f, 0.f, 0.f};
    D = mfma(tr_read(wp, S64), *(const u32x4*)xp, D);
    D = mfma(tr_read(wp + 32 * S64, S64), *(const u32x4*)(xp + 32), D);
    if (isq) {
      D = mfma(tr_read(wp + 64 * S64, S64), *(const u32x4*)(xp + 64), D);
      D = mfma(tr_read(wp + 96 * S64, S64), *(const u32x4*)(xp + 96), D);
    }
    const u32 slot = isq ? QP_OFF + (u32)(((kvh * 8 + ab) * 4 + aj) * 4096) : foob + HP_OFF + (u32)((c28 * 8 + fidx) * 4096);
    ST_SC(unit_of(D, lo, tag), rs_w, slot + (u32)((n & 7) * 512 + (8 * cb + 2 * g + (n >> 3)) * 16));
  }
  PIN();
  STAMP(2);

#pragma unroll
  for (int i = KPRE; i < 8; ++i) Kr[i] = LD_NT(rs_k, koff + (u32)((i >> 1) * 2048 + (i & 1) * 128));
  PIN();
  const u32 qoff = QP_OFF + (u32)(((kvh * 8 + wave) * 4 + 2 * (lane >> 5)) * 4096 + ab * 512 + (lane & 31) * 16);
  u32x4 qa = LD_SC(rs_w, qoff), qb = LD_SC(rs_w, qoff + 4096u);
  const u32 hoff = foob + HP_OFF + (u32)((c28 * 8 + 2 * g) * 4096 + wave * 512 + (fidx >> 2) * 256 + n * 16);
  u32x4 ha = LD_SC(rs_w, hoff), hb = LD_SC(rs_w, hoff + 4096u);
  u32 b1v = __builtin_amdgcn_raw_buffer_load_b32(rs_b1, foob + (u32)(64 * c28 + 32 * (fidx >> 2) + 2 * n) * 2u, 0, 0);
  PIN();
  u32x4 Vr[8];
  {
    const u32 voff = kvbase + (u32)(4 * g * 256 + n * 16);
#pragma unroll
    for (int e = 0; e < 8; ++e) Vr[e] = LD_NT(rs_v, voff + (u32)(((e & 3) + 16 * (e >> 2)) * 256));
  }
  PIN();
  {
    const u32 o = (u32)((kvh * 512 + 64 * ab + 8 * wave + g) * DM + 128 * aj + 8 * n) * 2u;
    Wt[0] = LD_NT(rs_wo, o);
    Wt[1] = LD_NT(rs_wo, o + (u32)(4 * DM) * 2u);
    Wt[2] = LD_NT(rs_w2, foob + (u32)((64 * c28 + 32 * (fidx >> 2) + 4 * wave + g) * DM + 128 * (fidx & 3) + 8 * n) * 2u);
  }
  PIN();

  if (!__all(tag_ok(qa, tag) & tag_ok(qb, tag))) {
#pragma nounroll
    for (int spin = 0; spin < SPIN_CAP; ++spin) {
      PIN();
      qa = LD_SC(rs_w, qoff); qb = LD_SC(rs_w, qoff + 4096u);
      if (__all(tag_ok(qa, tag) & tag_ok(qb, tag))) break;
    }
  }
  {
    float v0 = xor32_add(asf(qa[0]) + asf(qb[0]));
    float v1 = xor32_add(asf(qa[2]) + asf(qb[2]));
    if (lane < 32) *(u32*)&s_q[wave * 64 + lane * 2] = pack_bf(v0, v1);
  }
  __syncthreads();
  STAMP(3);

  float S[8];
  {
    const int qi = (n & 3) * 128 + ((n >> 2) & 1) * 32 + 8 * g;
    u32x4 B0 = *(const u32x4*)&s_q[qi], B1 = *(const u32x4*)&s_q[qi + 64];
    f32x4 D[4];
#pragma unroll
    for (int s = 0; s < 4; ++s) {
      D[s] = mfma(Kr[2 * s], B0, f32x4{0.f, 0.f, 0.f, 0.f});
      D[s] = mfma(Kr[2 * s + 1], B1, D[s]);
    }
#pragma unroll
    for (int p = 0; p < 2; ++p)
#pragma unroll
      for (int j = 0; j < 4; ++j) {
        auto a = __builtin_amdgcn_permlane32_swap(asu(D[2 * p][j]), asu(D[2 * p + 1][j]), false, false);
        float sc = asf(a[0]) + dpp<0x104>(asf(a[1]));
        S[p * 4 + j] = rbf(rbf(sc) * 0.08838834764831845f);
      }
  }
  {
    float m = fmaxf(fmaxf(fmaxf(S[0], S[1]), fmaxf(S[2], S[3])), fmaxf(fmaxf(S[4], S[5]), fmaxf(S[6], S[7])));
    m = xor32_max(xor16_max(m));
    if (lane < 4) s_m[wave * 4 + lane] = m;
  }
  __syncthreads();
  u32x4 P;
  float Mwg;
  {
    const int h = n & 3;
    float m = s_m[h];
#pragma unroll
    for (int w = 1; w < 8; ++w) m = fmaxf(m, s_m[w * 4 + h]);
    Mwg = m;
    float p[8], l = 0.f;
#pragma unroll
    for (int i = 0; i < 8; ++i) { p[i] = __expf(S[i] - m); l += p[i]; }
#pragma unroll
    for (int i = 0; i < 4; ++i) P[i] = pack_bf(p[2 * i], p[2 * i + 1]);
    l = xor32_add(xor16_add(l));
    if (lane < 4) s_l[wave * 4 + lane] = l;
  }
  if (ffn && !__all(tag_ok(ha, tag) & tag_ok(hb, tag))) {
#pragma nounroll
    for (int spin = 0; spin < SPIN_CAP; ++spin) {
      PIN();
      ha = LD_SC(rs_w, hoff); hb = LD_SC(rs_w, hoff + 4096u);
      if (__all(tag_ok(ha, tag) & tag_ok(hb, tag))) break;
    }
  }
  {
    float v0 = xor32_add(xor16_add(asf(ha[0]) + asf(hb[0])));
    float v1 = xor32_add(xor16_add(asf(ha[2]) + asf(hb[2])));
    float t[2] = {rbf(rbf(v0) + bf_lo(b1v)), rbf(rbf(v1) + bf_hi(b1v))};
    float hv[2];
#pragma unroll
    for (int i = 0; i < 2; ++i) {
      float u = 0.7978845608028654f * (t[i] + 0.044715f * t[i] * t[i] * t[i]);
      float th = 1.f - 2.f * __builtin_amdgcn_rcpf(__expf(2.f * u) + 1.f);
      hv[i] = 0.5f * t[i] * (1.f + th);
    }
    if (lane < 16) *(u32*)&s_hA[wave * HA_LD + 2 * lane] = pack_bf(hv[0], hv[1]);
  }
  STAMP(4);
  {
    f32x4 D[8];
#pragma unroll
    for (int e = 0; e < 8; ++e) D[e] = mfma(P, tr_col(Vr, e), f32x4{0.f, 0.f, 0.f, 0.f});
    if (lane < 16) {
#pragma unroll
      for (int j = 0; j < 4; ++j) {
        *(f32x4*)&s_o[wave * 512 + j * 128 + 8 * lane] = f32x4{D[0][j], D[1][j], D[2][j], D[3][j]};
        *(f32x4*)&s_o[wave * 512 + j * 128 + 8 * lane + 4] = f32x4{D[4][j], D[5][j], D[6][j], D[7][j]};
      }
    }
  }
  __syncthreads();
  STAMP(5);
  {
    const int u = tid >> 1;
    const float* sp = &s_o[(tid & 1) * 2048 + 2 * u];
    f32x2 a = (*(const f32x2*)sp + *(const f32x2*)(sp + 512)) + (*(const f32x2*)(sp + 1024) + *(const f32x2*)(sp + 1536));
    float a0 = a[0] + dpp<0xB1>(a[0]), a1 = a[1] + dpp<0xB1>(a[1]);
    float l = s_l[lane & 3];
#pragma unroll
    for (int w = 1; w < 8; ++w) l += s_l[w * 4 + (lane & 3)];
    u32x4 d = {asu(a0), tag, asu(a1), tag};
    u32 off = OP_OFF + (u32)((((kvh * 8 + (u >> 5)) * 8 + ab) * 4 + aj) * 512 + (u & 31) * 16);
    if (tid & 1) off = OOB;
    ST_SC(d, rs_w, off);
    u32x4 sd = {asu(Mwg), tag, asu(l), tag};
    ST_SC(sd, rs_w, tid < 4 ? OS_OFF + (u32)(((kvh * 8 + ab) * 4 + aj) * 64 + tid * 16) : OOB);
  }
  PIN();
  const u32 ooff = OP_OFF + (u32)(((kvh * 8 + ab) * 8 + wave) * 2048 + (lane >> 5) * 1024 + (lane & 31) * 16);
  const u32 soff = OS_OFF + (u32)((kvh * 8 + wave) * 256 + (lane >> 5) * 128 + (ab >> 1) * 16);
  u32x4 ov0 = LD_SC(rs_w, ooff), ov1 = LD_SC(rs_w, ooff + 512u), sv0 = LD_SC(rs_w, soff), sv1 = LD_SC(rs_w, soff + 64u);
  PIN();
  {
    char* p = s_wb + (8 * wave + g) * S128 + n * 16;
    *(u32x4*)p = Wt[0];
    *(u32x4*)(p + 4 * S128) = Wt[1];
    *(u32x4*)(s_wb + (64 + 4 * wave + g) * S128 + n * 16) = Wt[2];
  }
  __syncthreads();
  STAMP(6);
  const u32 puboff = (u32)(((n & 7) * 8 + wave) * 8192 + (2 * g + (n >> 3)) * 16);
  u32x4 A0 = tr_read(s_wb + wave * 32 + trl128, S128), A1 = tr_read(s_wb + 32 * S128 + wave * 32 + trl128, S128);
  {
    u32x4 A2 = tr_read(s_wb + 64 * S128 + wave * 32 + trl128, S128);
    f32x4 D = mfma(A2, *(const u32x4*)&s_hA[(n & 7) * HA_LD + 8 * g], f32x4{0.f, 0.f, 0.f, 0.f});
    ST_SC(unit_of(D, lo, tag), rs_w, foob + YP_OFF + (u32)((fidx & 3) * 64 * 8192 + (2 * c28 + (fidx >> 2)) * 128) + puboff);
  }
  PIN();
  if (!__all(tag_ok(ov0, tag) & tag_ok(ov1, tag) & tag_ok(sv0, tag) & tag_ok(sv1, tag))) {
#pragma nounroll
    for (int spin = 0; spin < SPIN_CAP; ++spin) {
      PIN();
      ov0 = LD_SC(rs_w, ooff); ov1 = LD_SC(rs_w, ooff + 512u); sv0 = LD_SC(rs_w, soff); sv1 = LD_SC(rs_w, soff + 64u);
      if (__all(tag_ok(ov0, tag) & tag_ok(ov1, tag) & tag_ok(sv0, tag) & tag_ok(sv1, tag))) break;
    }
  }
  {
    float M = xor32_max(fmaxf(asf(sv0[0]), asf(sv1[0])));
    float w0 = __expf(asf(sv0[0]) - M), w1v = __expf(asf(sv1[0]) - M);
    float L = xor32_add(w0 * asf(sv0[2]) + w1v * asf(sv1[2]));
    float o0 = xor32_add(w0 * asf(ov0[0]) + w1v * asf(ov1[0]));
    float o1 = xor32_add(w0 * asf(ov0[2]) + w1v * asf(ov1[2]));
    float inv = __builtin_amdgcn_rcpf(L);
    if (lane < 32) *(u32*)&s_oA[wave * OA_LD + 2 * lane] = pack_bf(o0 * inv, o1 * inv);
  }
  __syncthreads();
  STAMP(7);
  {
    const u16* op = &s_oA[(n & 7) * OA_LD + 8 * g];
    f32x4 D = mfma(A0, *(const u32x4*)op, f32x4{0.f, 0.f, 0.f, 0.f});
    D = mfma(A1, *(const u32x4*)(op + 32), D);
    ST_SC(unit_of(D, lo, tag), rs_w, SP_OFF + (u32)(aj * 64 * 8192 + (kvh * 8 + ab) * 128) + puboff);
  }
  PIN();

  {
    const u32 oidx = (u32)(((bid >> 3) & 7) * DM + 128 * (bid >> 6) + 16 * (bid & 7) + 2 * (lane & 7));
    u32 xw = __builtin_amdgcn_raw_buffer_load_b32(rs_x, oidx * 2u, 0, 0);
    u32 b2w = __builtin_amdgcn_raw_buffer_load_b32(rs_b2, (oidx & 511u) * 2u, 0, 0);
    const u32 foff = (u32)(bid * 8192 + tid * 16);
    const bool hasy = wave < 7;
    const u32 yoff = hasy ? YP_OFF + foff : OOB;
    u32x4 sv, yv;
#pragma nounroll
    for (int spin = 0; spin < SPIN_CAP; ++spin) {
      PIN();
      sv = LD_SC(rs_w, SP_OFF + foff);
      yv = LD_SC(rs_w, yoff);
      if (__all(tag_ok(sv, tag) & (tag_ok(yv, tag) | !hasy))) break;
    }
    float a[4] = {asf(sv[0]), asf(sv[2]), asf(yv[0]), asf(yv[2])};
#pragma unroll
    for (int i = 0; i < 4; ++i) { a[i] += dpp<0x128>(a[i]); a[i] = xor32_add(xor16_add(a[i])); }
    if (lane < 8) *(f32x4*)&s_red[wave * 32 + lane * 4] = f32x4{a[0], a[1], a[2], a[3]};
    __syncthreads();
    STAMP(8);
    if (tid < 8) {
      f32x4 t = *(const f32x4*)&s_red[tid * 4];
#pragma unroll
      for (int w = 1; w < 8; ++w) t += *(const f32x4*)&s_red[w * 32 + tid * 4];
      float o[2];
#pragma unroll
      for (int i = 0; i < 2; ++i) {
        float s = rbf(t[i]);
        float y = rbf(rbf(t[2 + i]) + (i ? bf_hi(b2w) : bf_lo(b2w)));
        float gt = rbf(y * __builtin_amdgcn_rcpf(1.f + __expf(-y)));
        o[i] = (i ? bf_hi(xw) : bf_lo(xw)) + rbf(s * gt);
      }
      *(u32*)(out + oidx) = pack_bf(o[0], o[1]);
      if (bid == 0 && tid == 0) __builtin_amdgcn_raw_buffer_store_b32(tag, rs_w, 0, 0, 16);
    }
  }
  STAMP(9);
}

}

extern "C" void kernel_launch(void* const* d_in, const int* in_sizes, int n_in, void* d_out, int out_size,
                              void* d_ws, size_t ws_size, hipStream_t stream) {
  fused_kernel<<<256, 512, 0, stream>>>((const u16*)d_in[0], (const u16*)d_in[1], (const u16*)d_in[2],
                                        (const u16*)d_in[3], (const u16*)d_in[4], (const u16*)d_in[6],
                                        (const u16*)d_in[7], (const u16*)d_in[8], (const u16*)d_in[9],
                                        (u16*)d_out, (char*)d_ws);
}
```

```cpp
#include <hip/hip_runtime.h>
#include <stdint.h>

typedef unsigned short u16;
typedef unsigned u32;
typedef __attribute__((ext_vector_type(4))) unsigned u32x4;
typedef __attribute__((ext_vector_type(2))) unsigned u32x2;
typedef __attribute__((ext_vector_type(2))) float f32x2;
typedef __attribute__((ext_vector_type(4))) float f32x4;
typedef __attribute__((ext_vector_type(8))) __bf16 bf16x8;
typedef __attribute__((ext_vector_type(2))) __bf16 bf16x2;
typedef __attribute__((ext_vector_type(4))) short s16x4;
#define LAS __attribute__((address_space(3)))

#ifndef NV1
#define NV1 4
#endif
#define PWAIT (NV1 + 1)
#define NFIN 4
#define STR2(x) #x
#define STR(x) STR2(x)

namespace {

constexpr int NB = 8, DM = 512, HQ = 32, HKV = 8, SK = 4096, DH = 128, WIN = 1024, FF = 1792;
constexpr u32 MB = 1u << 20;
constexpr u32 QP_OFF = 4096, HP_OFF = QP_OFF + MB, OP_OFF = HP_OFF + MB, OS_OFF = OP_OFF + MB,
              SP_OFF = OS_OFF + 65536, YP_OFF = SP_OFF + 2 * MB, WS_END = YP_OFF + 2 * MB;
constexpr u32 OOB = 0x7F000000u;
constexpr int SPIN_CAP = 400000;

#define LD_NT(rs, off) __builtin_amdgcn_raw_buffer_load_b128(rs, off, 0, 2)
#define LD_SC(rs, off) __builtin_amdgcn_raw_buffer_load_b128(rs, off, 0, 16)
#define ST_SC(v, rs, off) __builtin_amdgcn_raw_buffer_store_b128(v, rs, off, 0, 16)
#define PIN() asm volatile("" ::: "memory")

__device__ __forceinline__ float bf_lo(u32 v) { return __uint_as_float(v << 16); }
__device__ __forceinline__ float bf_hi(u32 v) { return __uint_as_float(v & 0xFFFF0000u); }
__device__ __forceinline__ float rbf(float f) { return (float)(__bf16)f; }
__device__ __forceinline__ u32 pack_bf(float lo, float hi) {
  f32x2 v = {lo, hi};
  return __builtin_bit_cast(u32, __builtin_convertvector(v, bf16x2));
}
__device__ __forceinline__ float asf(u32 v) { return __uint_as_float(v); }
__device__ __forceinline__ u32 asu(float v) { return __float_as_uint(v); }
template <int CTRL>
__device__ __forceinline__ float dpp(float v) {
  return __int_as_float(__builtin_amdgcn_update_dpp(0, __float_as_int(v), CTRL, 0xF, 0xF, true));
}
__device__ __forceinline__ float xor16_add(float v) {
  auto r = __builtin_amdgcn_permlane16_swap(asu(v), asu(v), false, false);
  return asf(r[0]) + asf(r[1]);
}
__device__ __forceinline__ float xor32_add(float v) {
  auto r = __builtin_amdgcn_permlane32_swap(asu(v), asu(v), false, false);
  return asf(r[0]) + asf(r[1]);
}
__device__ __forceinline__ float xor16_max(float v) {
  auto r = __builtin_amdgcn_permlane16_swap(asu(v), asu(v), false, false);
  return fmaxf(asf(r[0]), asf(r[1]));
}
__device__ __forceinline__ float xor32_max(float v) {
  auto r = __builtin_amdgcn_permlane32_swap(asu(v), asu(v), false, false);
  return fmaxf(asf(r[0]), asf(r[1]));
}
__device__ __forceinline__ float wave_sum(float v) {
  v += dpp<0xB1>(v);
  v += dpp<0x4E>(v);
  v += dpp<0x141>(v);
  v += dpp<0x140>(v);
  return xor32_add(xor16_add(v));
}
__device__ __forceinline__ bf16x8 as_bf(u32x4 v) { return __builtin_bit_cast(bf16x8, v); }
__device__ __forceinline__ f32x4 mfma(u32x4 a, u32x4 b, f32x4 c) {
  return __builtin_amdgcn_mfma_f32_16x16x32_bf16(as_bf(a), as_bf(b), c, 0, 0, 0);
}
__device__ __forceinline__ u32x4 tr_col(const u32x4* R, int e) {
  const int d = e >> 1;
  const u32 sel = (e & 1) ? 0x07060302u : 0x05040100u;
  u32x4 o;
  o[0] = __builtin_amdgcn_perm(R[1][d], R[0][d], sel);
  o[1] = __builtin_amdgcn_perm(R[3][d], R[2][d], sel);
  o[2] = __builtin_amdgcn_perm(R[5][d], R[4][d], sel);
  o[3] = __builtin_amdgcn_perm(R[7][d], R[6][d], sel);
  return o;
}
__device__ __forceinline__ u32x4 tr_read(const char* p, int stride) {
  s16x4 a = __builtin_amdgcn_ds_read_tr16_b64_v4i16((LAS s16x4*)p);
  s16x4 b = __builtin_amdgcn_ds_read_tr16_b64_v4i16((LAS s16x4*)(p + 4 * stride));
  u32x2 ua = __builtin_bit_cast(u32x2, a), ub = __builtin_bit_cast(u32x2, b);
  return u32x4{ua[0], ua[1], ub[0], ub[1]};
}
__device__ __forceinline__ bool tag_ok(const u32x4& v, u32 tag) { return (v[1] == tag) & (v[3] == tag); }
__device__ __forceinline__ u32x4 unit_of(const f32x4& D, bool lo, u32 tag) {
  return u32x4{asu(lo ? D[0] : D[2]), tag, asu(lo ? D[1] : D[3]), tag};
}

#define STAMP(i) do { } while (0)

constexpr int XN_LD = 520;
constexpr int OA_LD = 72;
constexpr int HA_LD = 40;
constexpr int S64 = 160;
constexpr int S128 = 288;

__global__ void __launch_bounds__(512, 2)
fused_kernel(const u16* __restrict__ x, const u16* __restrict__ kc, const u16* __restrict__ vc,
             const u16* __restrict__ wq, const u16* __restrict__ wo, const u16* __restrict__ w1,
             const u16* __restrict__ b1, const u16* __restrict__ w2, const u16* __restrict__ b2,
             u16* out, char* ws) {
  __shared__ __attribute__((aligned(16))) char s_wa[192 * S64];
  __shared__ __attribute__((aligned(16))) char s_wb[96 * S128];
  __shared__ __attribute__((aligned(16))) u16 s_xn[8 * XN_LD];
  __shared__ __attribute__((aligned(16))) u16 s_q[512];
  __shared__ __attribute__((aligned(16))) u16 s_oA[8 * OA_LD];
  __shared__ __attribute__((aligned(16))) u16 s_hA[8 * HA_LD];
  __shared__ __attribute__((aligned(16))) float s_m[8 * 4];
  __shared__ __attribute__((aligned(16))) float s_l[8 * 4];
  __shared__ __attribute__((aligned(16))) float s_o[8 * 512];

  const int bid = blockIdx.x, tid = threadIdx.x, lane = tid & 63;
  const int wave = __builtin_amdgcn_readfirstlane(tid >> 6);
  const int kvh = bid & 7, r = bid >> 3;
  const int ab = r >> 2, aj = r & 3;
  const bool ffn = bid < 224;
  const int c28 = bid >> 3, fidx = bid & 7;
  const u32 foob = ffn ? 0u : OOB;
  const int n = lane & 15, g = lane >> 4;
  const bool lo = n < 8;
  STAMP(0);

  auto rs_x = __builtin_amdgcn_make_buffer_rsrc((void*)x, 0, NB * DM * 2, 0x00020000);
  auto rs_k = __builtin_amdgcn_make_buffer_rsrc((void*)kc, 0, NB * HKV * SK * DH * 2, 0x00020000);
  auto rs_v = __builtin_amdgcn_make_buffer_rsrc((void*)vc, 0, NB * HKV * SK * DH * 2, 0x00020000);
  auto rs_wq = __builtin_amdgcn_make_buffer_rsrc((void*)wq, 0, DM * HQ * DH * 2, 0x00020000);
  auto rs_wo = __builtin_amdgcn_make_buffer_rsrc((void*)wo, 0, DM * HQ * DH * 2, 0x00020000);
  auto rs_w1 = __builtin_amdgcn_make_buffer_rsrc((void*)w1, 0, DM * FF * 2, 0x00020000);
  auto rs_w2 = __builtin_amdgcn_make_buffer_rsrc((void*)w2, 0, DM * FF * 2, 0x00020000);
  auto rs_b1 = __builtin_amdgcn_make_buffer_rsrc((void*)b1, 0, FF * 2, 0x00020000);
  auto rs_b2 = __builtin_amdgcn_make_buffer_rsrc((void*)b2, 0, DM * 2, 0x00020000);
  auto rs_w = __builtin_amdgcn_make_buffer_rsrc((void*)ws, 0, WS_END, 0x00020000);

  u32x4 xv = __builtin_amdgcn_raw_buffer_load_b128(rs_x, (u32)(wave * DM + lane * 8) * 2u, 0, 0);
  u32 epoch = __builtin_amdgcn_raw_buffer_load_b32(rs_w, 0, 0, 16);
  u32x4 Wt[3];
  {
    const u32 o = (u32)((128 * aj + 16 * wave + (lane >> 3)) * (HQ * DH) + kvh * 512 + 64 * ab + 8 * (lane & 7)) * 2u;
    Wt[0] = LD_NT(rs_wq, o);
    Wt[1] = LD_NT(rs_wq, o + (u32)(8 * HQ * DH) * 2u);
    Wt[2] = LD_NT(rs_w1, foob + (u32)((64 * fidx + 8 * wave + (lane >> 3)) * FF + 64 * c28 + 8 * (lane & 7)) * 2u);
  }
  PIN();
  __builtin_amdgcn_s_barrier();
  PIN();
  const u32 kvbase = (u32)(((ab * HKV + kvh) * SK + (SK - WIN) + aj * 256 + wave * 32) * DH) * 2u;
  const u32 koff = kvbase + (u32)((n & 7) * 256 + (n >> 3) * 64 + g * 16);
  u32x4 Kr[8];
#pragma unroll
  for (int i = 0; i < 8; ++i) Kr[i] = LD_NT(rs_k, koff + (u32)((i >> 1) * 2048 + (i & 1) * 128));
  PIN();

  {
    float v[8];
#pragma unroll
    for (int i = 0; i < 4; ++i) { v[2 * i] = bf_lo(xv[i]); v[2 * i + 1] = bf_hi(xv[i]); }
    float s = ((v[0] + v[1]) + (v[2] + v[3])) + ((v[4] + v[5]) + (v[6] + v[7]));
    float mean = wave_sum(s) * (1.f / DM);
    float q = 0.f;
#pragma unroll
    for (int i = 0; i < 8; ++i) { float d = v[i] - mean; q += d * d; }
    float var = rbf(wave_sum(q) * (1.f / DM));
    float mb = rbf(mean);
    float den = rbf(sqrtf(rbf(var + 1e-5f)));
    float inv = 1.f / den;
    u32x4 o;
#pragma unroll
    for (int i = 0; i < 4; ++i) {
      u32 d = pack_bf(v[2 * i] - mb, v[2 * i + 1] - mb);
      o[i] = pack_bf(bf_lo(d) * inv, bf_hi(d) * inv);
    }
    *(u32x4*)&s_xn[wave * XN_LD + lane * 8] = o;
  }
  const u32 tag = __builtin_amdgcn_readfirstlane(epoch) + 0x9E3779B9u;
  {
    char* p = s_wa + (16 * wave + (lane >> 3)) * S64 + (lane & 7) * 16;
    *(u32x4*)p = Wt[0];
    *(u32x4*)(p + 8 * S64) = Wt[1];
    *(u32x4*)(s_wa + (128 + 8 * wave + (lane >> 3)) * S64 + (lane & 7) * 16) = Wt[2];
  }
  __syncthreads();
  STAMP(1);

  const int trl64 = (8 * g + (n >> 2)) * S64 + (n & 3) * 8;
  const int trl128 = (8 * g + (n >> 2)) * S128 + (n & 3) * 8;
  {
    const bool isq = wave < 4;
    const int cb = wave & 3;
    const char* wp = s_wa + (isq ? 0 : 128 * S64) + cb * 32 + trl64;
    const u16* xp = &s_xn[(n & 7) * XN_LD + (isq ? 128 * aj : 64 * fidx) + 8 * g];
    f32x4 D = {0.f, 0.f, 0.f, 0.f};
    D = mfma(tr_read(wp, S64), *(const u32x4*)xp, D);
    D = mfma(tr_read(wp + 32 * S64, S64), *(const u32x4*)(xp + 32), D);
    if (isq) {
      D = mfma(tr_read(wp + 64 * S64, S64), *(const u32x4*)(xp + 64), D);
      D = mfma(tr_read(wp + 96 * S64, S64), *(const u32x4*)(xp + 96), D);
    }
    const u32 slot = isq ? QP_OFF + (u32)(((kvh * 8 + ab) * 4 + aj) * 4096) : foob + HP_OFF + (u32)((c28 * 8 + fidx) * 4096);
    ST_SC(unit_of(D, lo, tag), rs_w, slot + (u32)((n & 7) * 512 + (8 * cb + 2 * g + (n >> 3)) * 16));
  }
  PIN();
  __builtin_amdgcn_s_barrier();
  PIN();
  STAMP(2);

  u32x4 Vr[8];
  const u32 voff = kvbase + (u32)(4 * g * 256 + n * 16);
#pragma unroll
  for (int e = 0; e < NV1; ++e) Vr[e] = LD_NT(rs_v, voff + (u32)(((e & 3) + 16 * (e >> 2)) * 256));
  const u32 qoff = QP_OFF + (u32)(((kvh * 8 + wave) * 4 + 2 * (lane >> 5)) * 4096 + ab * 512 + (lane & 31) * 16);
  const u32 hoff = foob + HP_OFF + (u32)((c28 * 8 + 2 * g) * 4096 + wave * 512 + (fidx >> 2) * 256 + n * 16);
  const u32 wooff = (u32)((kvh * 512 + 64 * ab + 8 * wave + g) * DM + 128 * aj + 8 * n) * 2u;
  const u32 w2off = foob + (u32)((64 * c28 + 32 * (fidx >> 2) + 4 * wave + g) * DM + 128 * (fidx & 3) + 8 * n) * 2u;
  asm volatile("s_waitcnt vmcnt(" STR(PWAIT) ")" ::: "memory");
  u32x4 qa = LD_SC(rs_w, qoff), qb = LD_SC(rs_w, qoff + 4096u);
  PIN();
#pragma unroll
  for (int e = NV1; e < 8; ++e) Vr[e] = LD_NT(rs_v, voff + (u32)(((e & 3) + 16 * (e >> 2)) * 256));
  PIN();
  Wt[0] = LD_NT(rs_wo, wooff);
  Wt[1] = LD_NT(rs_wo, wooff + (u32)(4 * DM) * 2u);
  Wt[2] = LD_NT(rs_w2, w2off);
  PIN();

  if (!__all(tag_ok(qa, tag) & tag_ok(qb, tag))) {
#pragma nounroll
    for (int spin = 0; spin < SPIN_CAP; ++spin) {
      PIN();
      qa = LD_SC(rs_w, qoff); qb = LD_SC(rs_w, qoff + 4096u);
      if (__all(tag_ok(qa, tag) & tag_ok(qb, tag))) break;
    }
  }
  {
    float v0 = xor32_add(asf(qa[0]) + asf(qb[0]));
    float v1 = xor32_add(asf(qa[2]) + asf(qb[2]));
    if (lane < 32) *(u32*)&s_q[wave * 64 + lane * 2] = pack_bf(v0, v1);
  }
  __syncthreads();
  STAMP(3);

  float S[8];
  {
    const int qi = (n & 3) * 128 + ((n >> 2) & 1) * 32 + 8 * g;
    u32x4 B0 = *(const u32x4*)&s_q[qi], B1 = *(const u32x4*)&s_q[qi + 64];
    f32x4 D[4];
#pragma unroll
    for (int s = 0; s < 4; ++s) {
      D[s] = mfma(Kr[2 * s], B0, f32x4{0.f, 0.f, 0.f, 0.f});
      D[s] = mfma(Kr[2 * s + 1], B1, D[s]);
    }
#pragma unroll
    for (int p = 0; p < 2; ++p)
#pragma unroll
      for (int j = 0; j < 4; ++j) {
        auto a = __builtin_amdgcn_permlane32_swap(asu(D[2 * p][j]), asu(D[2 * p + 1][j]), false, false);
        float sc = asf(a[0]) + dpp<0x104>(asf(a[1]));
        S[p * 4 + j] = rbf(rbf(sc) * 0.08838834764831845f);
      }
  }
  {
    float m = fmaxf(fmaxf(fmaxf(S[0], S[1]), fmaxf(S[2], S[3])), fmaxf(fmaxf(S[4], S[5]), fmaxf(S[6], S[7])));
    m = xor32_max(xor16_max(m));
    if (lane < 4) s_m[wave * 4 + lane] = m;
  }
  __syncthreads();
  u32x4 P;
  float Mwg;
  {
    const int h = n & 3;
    float m = s_m[h];
#pragma unroll
    for (int w = 1; w < 8; ++w) m = fmaxf(m, s_m[w * 4 + h]);
    Mwg = m;
    float p[8], l = 0.f;
#pragma unroll
    for (int i = 0; i < 8; ++i) { p[i] = __expf(S[i] - m); l += p[i]; }
#pragma unroll
    for (int i = 0; i < 4; ++i) P[i] = pack_bf(p[2 * i], p[2 * i + 1]);
    l = xor32_add(xor16_add(l));
    if (lane < 4) s_l[wave * 4 + lane] = l;
  }
  u32x4 ha = LD_SC(rs_w, hoff), hb = LD_SC(rs_w, hoff + 4096u);
  u32 b1v = __builtin_amdgcn_raw_buffer_load_b32(rs_b1, foob + (u32)(64 * c28 + 32 * (fidx >> 2) + 2 * n) * 2u, 0, 0);
  __syncthreads();
  {
    float l = s_l[lane & 3];
#pragma unroll
    for (int w = 1; w < 8; ++w) l += s_l[w * 4 + (lane & 3)];
    u32x4 sd = {asu(Mwg), tag, asu(l), tag};
    ST_SC(sd, rs_w, tid < 4 ? OS_OFF + (u32)(((kvh * 8 + ab) * 4 + aj) * 64 + tid * 16) : OOB);
  }
  PIN();
  STAMP(4);
  const u32 opub = (tid & 1) ? OOB : OP_OFF + (u32)((((kvh * 8 + (tid >> 6)) * 8 + ab) * 4 + aj) * 512 + ((tid >> 1) & 31) * 16);
  const u32 ooff = OP_OFF + (u32)(((kvh * 8 + ab) * 8 + wave) * 2048 + (lane >> 5) * 1024 + (lane & 31) * 16);
  const u32 soff = OS_OFF + (u32)((kvh * 8 + wave) * 256 + (lane >> 5) * 128 + (ab >> 1) * 16);
  const u32 puboff = (u32)(((n & 7) * 8 + wave) * 8192 + (2 * g + (n >> 3)) * 16);
  const u32 ypub = foob + YP_OFF + (u32)((fidx & 3) * 64 * 8192 + (2 * c28 + (fidx >> 2)) * 128) + puboff;
  const u32 spub = SP_OFF + (u32)(aj * 64 * 8192 + (kvh * 8 + ab) * 128) + puboff;
  {
    u32x4 T[8];
#pragma unroll
    for (int p = 0; p < 4; ++p) {
#pragma unroll
      for (int e = 0; e < 8; ++e)
        T[e][p] = __builtin_amdgcn_perm(Vr[2 * p + 1][e >> 1], Vr[2 * p][e >> 1], (e & 1) ? 0x07060302u : 0x05040100u);
      __builtin_amdgcn_sched_barrier(0);
    }
    f32x4 D[8];
#pragma unroll
    for (int e = 0; e < 8; ++e) D[e] = mfma(P, T[e], f32x4{0.f, 0.f, 0.f, 0.f});
    if (lane < 16) {
#pragma unroll
      for (int j = 0; j < 4; ++j) {
        *(f32x4*)&s_o[wave * 512 + j * 128 + 8 * lane] = f32x4{D[0][j], D[1][j], D[2][j], D[3][j]};
        *(f32x4*)&s_o[wave * 512 + j * 128 + 8 * lane + 4] = f32x4{D[4][j], D[5][j], D[6][j], D[7][j]};
      }
    }
  }
  __syncthreads();
  STAMP(5);
  {
    const float* sp = &s_o[(tid & 1) * 2048 + (tid & ~1)];
    f32x2 a = (*(const f32x2*)sp + *(const f32x2*)(sp + 512)) + (*(const f32x2*)(sp + 1024) + *(const f32x2*)(sp + 1536));
    float a0 = a[0] + dpp<0xB1>(a[0]), a1 = a[1] + dpp<0xB1>(a[1]);
    u32x4 d = {asu(a0), tag, asu(a1), tag};
    ST_SC(d, rs_w, opub);
  }
  PIN();
  u32x4 sv0 = LD_SC(rs_w, soff), sv1 = LD_SC(rs_w, soff + 64u);
  PIN();
  {
    char* p = s_wb + (8 * wave + g) * S128 + n * 16;
    *(u32x4*)p = Wt[0];
    *(u32x4*)(p + 4 * S128) = Wt[1];
    *(u32x4*)(s_wb + (64 + 4 * wave + g) * S128 + n * 16) = Wt[2];
  }
  if (ffn && !__all(tag_ok(ha, tag) & tag_ok(hb, tag))) {
#pragma nounroll
    for (int spin = 0; spin < SPIN_CAP; ++spin) {
      PIN();
      ha = LD_SC(rs_w, hoff); hb = LD_SC(rs_w, hoff + 4096u);
      if (__all(tag_ok(ha, tag) & tag_ok(hb, tag))) break;
    }
  }
  {
    float v0 = xor32_add(xor16_add(asf(ha[0]) + asf(hb[0])));
    float v1 = xor32_add(xor16_add(asf(ha[2]) + asf(hb[2])));
    float t[2] = {rbf(rbf(v0) + bf_lo(b1v)), rbf(rbf(v1) + bf_hi(b1v))};
    float hv[2];
#pragma unroll
    for (int i = 0; i < 2; ++i) {
      float u = 0.7978845608028654f * (t[i] + 0.044715f * t[i] * t[i] * t[i]);
      float th = 1.f - 2.f * __builtin_amdgcn_rcpf(__expf(2.f * u) + 1.f);
      hv[i] = 0.5f * t[i] * (1.f + th);
    }
    if (lane < 16) *(u32*)&s_hA[wave * HA_LD + 2 * lane] = pack_bf(hv[0], hv[1]);
  }
  __syncthreads();
  STAMP(6);
  u32x4 ov0 = LD_SC(rs_w, ooff), ov1 = LD_SC(rs_w, ooff + 512u);
  PIN();
  u32x4 A0 = tr_read(s_wb + wave * 32 + trl128, S128), A1 = tr_read(s_wb + 32 * S128 + wave * 32 + trl128, S128);
  {
    u32x4 A2 = tr_read(s_wb + 64 * S128 + wave * 32 + trl128, S128);
    f32x4 D = mfma(A2, *(const u32x4*)&s_hA[(n & 7) * HA_LD + 8 * g], f32x4{0.f, 0.f, 0.f, 0.f});
    ST_SC(unit_of(D, lo, tag), rs_w, ypub);
  }
  PIN();
  if (!__all(tag_ok(sv0, tag) & tag_ok(sv1, tag))) {
#pragma nounroll
    for (int spin = 0; spin < SPIN_CAP; ++spin) {
      PIN();
      sv0 = LD_SC(rs_w, soff); sv1 = LD_SC(rs_w, soff + 64u);
      if (__all(tag_ok(sv0, tag) & tag_ok(sv1, tag))) break;
    }
  }
  float w0, w1v;
  {
    float M = xor32_max(fmaxf(asf(sv0[0]), asf(sv1[0])));
    w0 = __expf(asf(sv0[0]) - M); w1v = __expf(asf(sv1[0]) - M);
    float inv = __builtin_amdgcn_rcpf(xor32_add(w0 * asf(sv0[2]) + w1v * asf(sv1[2])));
    w0 *= inv; w1v *= inv;
  }
  if (!__all(tag_ok(ov0, tag) & tag_ok(ov1, tag))) {
#pragma nounroll
    for (int spin = 0; spin < SPIN_CAP; ++spin) {
      PIN();
      ov0 = LD_SC(rs_w, ooff); ov1 = LD_SC(rs_w, ooff + 512u);
      if (__all(tag_ok(ov0, tag) & tag_ok(ov1, tag))) break;
    }
  }
  {
    float o0 = xor32_add(w0 * asf(ov0[0]) + w1v * asf(ov1[0]));
    float o1 = xor32_add(w0 * asf(ov0[2]) + w1v * asf(ov1[2]));
    if (lane < 32) *(u32*)&s_oA[wave * OA_LD + 2 * lane] = pack_bf(o0, o1);
  }
  __syncthreads();
  STAMP(7);
  {
    const u16* op = &s_oA[(n & 7) * OA_LD + 8 * g];
    f32x4 D = mfma(A0, *(const u32x4*)op, f32x4{0.f, 0.f, 0.f, 0.f});
    D = mfma(A1, *(const u32x4*)(op + 32), D);
    ST_SC(unit_of(D, lo, tag), rs_w, spub);
  }
  PIN();

  if (wave < NFIN) {
    const u32 oidx = (u32)(((bid >> 3) & 7) * DM + 128 * (bid >> 6) + 16 * (bid & 7) + 2 * (lane & 7));
    u32 xw = __builtin_amdgcn_raw_buffer_load_b32(rs_x, oidx * 2u, 0, 0);
    u32 b2w = __builtin_amdgcn_raw_buffer_load_b32(rs_b2, (oidx & 511u) * 2u, 0, 0);
    const u32 foff = (u32)(bid * 8192 + lane * 16);
    u32x4 v[8];
#pragma nounroll
    for (int spin = 0; spin < SPIN_CAP; ++spin) {
      PIN();
      bool ok = true;
#pragma unroll
      for (int i = 0; i < 7; ++i) v[i] = LD_SC(rs_w, YP_OFF + foff + i * 1024u);
#pragma unroll
      for (int i = 0; i < 7; ++i) ok &= tag_ok(v[i], tag);
      if (__all(ok)) break;
    }
    float gt[2];
#pragma unroll
    for (int c = 0; c < 2; ++c) {
      float a = ((asf(v[0][2 * c]) + asf(v[1][2 * c])) + (asf(v[2][2 * c]) + asf(v[3][2 * c]))) + ((asf(v[4][2 * c]) + asf(v[5][2 * c])) + asf(v[6][2 * c]));
      a += dpp<0x128>(a); a = xor32_add(xor16_add(a));
      float y = rbf(rbf(a) + (c ? bf_hi(b2w) : bf_lo(b2w)));
      gt[c] = rbf(y * __builtin_amdgcn_rcpf(1.f + __expf(-y)));
    }
    STAMP(8);
#pragma nounroll
    for (int spin = 0; spin < SPIN_CAP; ++spin) {
      PIN();
      bool ok = true;
#pragma unroll
      for (int i = 0; i < 8; ++i) v[i] = LD_SC(rs_w, SP_OFF + foff + i * 1024u);
#pragma unroll
      for (int i = 0; i < 8; ++i) ok &= tag_ok(v[i], tag);
      if (__all(ok)) break;
    }
    float o[2];
#pragma unroll
    for (int c = 0; c < 2; ++c) {
      float a = ((asf(v[0][2 * c]) + asf(v[1][2 * c])) + (asf(v[2][2 * c]) + asf(v[3][2 * c]))) + ((asf(v[4][2 * c]) + asf(v[5][2 * c])) + (asf(v[6][2 * c]) + asf(v[7][2 * c])));
      a += dpp<0x128>(a); a = xor32_add(xor16_add(a));
      o[c] = (c ? bf_hi(xw) : bf_lo(xw)) + rbf(rbf(a) * gt[c]);
    }
    if (lane < 8) *(u32*)(out + oidx) = pack_bf(o[0], o[1]);
    if (bid == 0 && tid == 0) __builtin_amdgcn_raw_buffer_store_b32(tag, rs_w, 0, 0, 16);
  } else { STAMP(8); }
  STAMP(9);
}

}

extern "C" void kernel_launch(void* const* d_in, const int* in_sizes, int n_in, void* d_out, int out_size,
                              void* d_ws, size_t ws_size, hipStream_t stream) {
  fused_kernel<<<256, 512, 0, stream>>>((const u16*)d_in[0], (const u16*)d_in[1], (const u16*)d_in[2],
                                        (const u16*)d_in[3], (const u16*)d_in[4], (const u16*)d_in[6],
                                        (const u16*)d_in[7], (const u16*)d_in[8], (const u16*)d_in[9],
                                        (u16*)d_out, (char*)d_ws);
}
```

```cpp
#define ARR 2
#define RX 4
#define STO 12
#define QO 1
#define QREF 1
#define WT2 8,8,8,8,8
#define QB0 0
#define NA 8,8,8,8,8,8,8,8
#define KV2 1
#define MRG 60
#define W1E 2
#define WOL 1
#define STE 1
#define W2TIME 0
#define QT 360
#define HT 360
#define WOA 3
#define CPFL 95
#include <hip/hip_runtime.h>
#include <stdint.h>

typedef unsigned short u16;
typedef unsigned u32;
typedef __attribute__((ext_vector_type(4))) unsigned u32x4;
typedef __attribute__((ext_vector_type(2))) unsigned u32x2;
typedef __attribute__((ext_vector_type(2))) float f32x2;
typedef __attribute__((ext_vector_type(4))) float f32x4;
typedef __attribute__((ext_vector_type(8))) __bf16 bf16x8;
typedef __attribute__((ext_vector_type(2))) __bf16 bf16x2;
typedef __attribute__((ext_vector_type(4))) short s16x4;
#define LAS __attribute__((address_space(3)))

#define VT 0
#define WFL 0
#define WQX 0
#define HQ1 4
#define O16 1
#define WIND 3
#define VTRIG 8
#define QDELAY 20
#define ACKW 1
#define QX 8
#define WOT 9
#define W1T 0
#define W2T 9
#define HDELAY 30
#define YT 6
#define CK1 3
#define CK2 6
#define ODELAY 0
#define OACK 1
#define SACK 1
#define PSLEEP 0
#define SEPOCH 0
#define KFIRST 0
#define PRE 0

#define LOCO 1
#define LSLEEP 0
#define SPRE 0
#define SAUX 16
#define STAUX 16
#define CPF 16
#define XR (8 * (sw + r))
#define VC 0
#define NB0 4
#define WT 16, 16, 16, 16, 1, 16, 1, 16, 2, 2, 2, 2, 2, 2, 2, 2
#define HB 0
#define QCAP 48
#define VFIRST 0
#define YLOC 1
#define ASMKV 1
#define ALLCK 1
#define WIND2 4
#define WTIME 0
#define FEXIT 1
#define SFULL 1
#define YST 0
#define SDELAY 0
#define EAUX 16
#define WMAX 0
#define WOP 4
#define WOB 3
#define PLW "s_waitcnt lgkmcnt(0)\n\t"
#define ESC " sc1"
#define EOFF 0
#define CPD 0
#define CPW "4"
#define PRO 1
#define EPG 1
#define GD 0
#define GT0 330
#define OX 1
#define YX 0
#define PLD 0
#define OST 0
#define VSL 0
#define VSR 0
#define VSX 0
#define VNT " nt"
#define ABX 0
#define QHACK(c) (c)
#define OLATE 0
#define SLATE 0
#define NBQ 0
#define SPIPE 0
#define FSL 1
#define SNP 1
#define SPD 8
#define ONP 1
#define OPD 4
#define OPD1 2
#define MPOS 3
namespace {

constexpr int NB = 8, DM = 512, HQ = 32, HKV = 8, SK = 4096, DH = 128, WIN = 1024, FF = 1792;
constexpr u32 KB = 1024u;
constexpr u32 QP_OFF = 4096, HP_OFF = QP_OFF + 512 * KB, OP_OFF = HP_OFF + 256 * KB, OS_OFF = OP_OFF + 512 * KB,
              SP_OFF = OS_OFF + 32 * KB, YP_OFF = SP_OFF + 1024 * KB, QB_OFF = YP_OFF + 512 * KB, WS_END = QB_OFF + 768 * KB;
constexpr u32 OOB = 0x7F000000u;
constexpr int SPIN_CAP = 400000;
constexpr int WTB[16] = {WT}, WTC[5] = {WT2}, NAB[8] = {NA};
constexpr int lim_of(int c) { return c + NAB[c] > 8 ? 8 : c + NAB[c]; }
constexpr bool k_safe() {
  for (int c = QB0; c < 8; ++c) {
    bool cov = false;
    for (int e = 0; e < c; ++e) cov |= WTB[8 + e] <= e;
    if (!cov && lim_of(c) != 8) return false;
    if (lim_of(c) < 3) return false;
  }
  return true;
}

#define LD_NT(rs, off) __builtin_amdgcn_raw_buffer_load_b128(rs, off, 0, 2)
#define LD_SC(rs, off) __builtin_amdgcn_raw_buffer_load_b128(rs, off, 0, 16)
#define LD_SC2(rs, off) __builtin_amdgcn_raw_buffer_load_b64(rs, off, 0, 16)
#define ST_SC(v, rs, off) __builtin_amdgcn_raw_buffer_store_b128(v, rs, off, 0, 16)
#define PIN() asm volatile("" ::: "memory")
#define WAITV(n) asm volatile("s_waitcnt vmcnt(%0)" ::"n"(n) : "memory")

__device__ __forceinline__ float bf_lo(u32 v) { return __uint_as_float(v << 16); }
__device__ __forceinline__ float bf_hi(u32 v) { return __uint_as_float(v & 0xFFFF0000u); }
__device__ __forceinline__ float rbf(float f) { return (float)(__bf16)f; }
__device__ __forceinline__ u32 pack_bf(float lo, float hi) {
  f32x2 v = {lo, hi};
  return __builtin_bit_cast(u32, __builtin_convertvector(v, bf16x2));
}
__device__ __forceinline__ float asf(u32 v) { return __uint_as_float(v); }
__device__ __forceinline__ u32 asu(float v) { return __float_as_uint(v); }
template <int CTRL>
__device__ __forceinline__ float dpp(float v) {
  return __int_as_float(__builtin_amdgcn_update_dpp(0, __float_as_int(v), CTRL, 0xF, 0xF, true));
}
__device__ __forceinline__ float xor16_add(float v) {
  auto r = __builtin_amdgcn_permlane16_swap(asu(v), asu(v), false, false);
  return asf(r[0]) + asf(r[1]);
}
__device__ __forceinline__ float xor32_add(float v) {
  auto r = __builtin_amdgcn_permlane32_swap(asu(v), asu(v), false, false);
  return asf(r[0]) + asf(r[1]);
}
__device__ __forceinline__ float xor16_max(float v) {
  auto r = __builtin_amdgcn_permlane16_swap(asu(v), asu(v), false, false);
  return fmaxf(asf(r[0]), asf(r[1]));
}
__device__ __forceinline__ float xor32_max(float v) {
  auto r = __builtin_amdgcn_permlane32_swap(asu(v), asu(v), false, false);
  return fmaxf(asf(r[0]), asf(r[1]));
}
__device__ __forceinline__ float half_sum(float v) {
  v += dpp<0xB1>(v);
  v += dpp<0x4E>(v);
  v += dpp<0x141>(v);
  v += dpp<0x140>(v);
  return xor16_add(v);
}
__device__ __forceinline__ float rows_scatter(float a0, float a1, float a2, float a3) {
  auto p = __builtin_amdgcn_permlane32_swap(asu(a0), asu(a2), false, false);
  auto q = __builtin_amdgcn_permlane32_swap(asu(a1), asu(a3), false, false);
  auto r = __builtin_amdgcn_permlane16_swap(asu(asf(p[0]) + asf(p[1])), asu(asf(q[0]) + asf(q[1])), false, false);
  return asf(r[0]) + asf(r[1]);
}
__device__ __forceinline__ bf16x8 as_bf(u32x4 v) { return __builtin_bit_cast(bf16x8, v); }
__device__ __forceinline__ f32x4 mfma(u32x4 a, u32x4 b, f32x4 c) {
  return __builtin_amdgcn_mfma_f32_16x16x32_bf16(as_bf(a), as_bf(b), c, 0, 0, 0);
}
__device__ __forceinline__ u32x4 tr_read(const char* p, int stride) {
  s16x4 a = __builtin_amdgcn_ds_read_tr16_b64_v4i16((LAS s16x4*)p);
  s16x4 b = __builtin_amdgcn_ds_read_tr16_b64_v4i16((LAS s16x4*)(p + 4 * stride));
  u32x2 ua = __builtin_bit_cast(u32x2, a), ub = __builtin_bit_cast(u32x2, b);
  return u32x4{ua[0], ua[1], ub[0], ub[1]};
}
__device__ __forceinline__ u32x4 tr_read_a(u32 p, int stride) {
  s16x4 a = __builtin_amdgcn_ds_read_tr16_b64_v4i16((LAS s16x4*)(uintptr_t)p);
  s16x4 b = __builtin_amdgcn_ds_read_tr16_b64_v4i16((LAS s16x4*)(uintptr_t)(p + 4 * stride));
  u32x2 ua = __builtin_bit_cast(u32x2, a), ub = __builtin_bit_cast(u32x2, b);
  return u32x4{ua[0], ua[1], ub[0], ub[1]};
}
__device__ __forceinline__ u32 enc(float v, u32 t) { return ((asu(v) + 0x400u) & 0xFFFFF800u) | t; }
__device__ __forceinline__ float dec(u32 v) { return asf(v & 0xFFFFF800u); }
__device__ __forceinline__ u32x4 enc4(const f32x4& D, u32 t0, u32 t1) {
  return u32x4{enc(D[0], t0), enc(D[1], t1), enc(D[2], t0), enc(D[3], t1)};
}
__device__ __forceinline__ u32 tdiff(const u32x4& v, u32 t0, u32 t1) {
  return ((v[0] ^ t0) | (v[1] ^ t1)) | ((v[2] ^ t0) | (v[3] ^ t1));
}
__device__ __forceinline__ bool tbad(u32 d) { return (d & 0x7FFu) != 0u; }
__device__ __forceinline__ u32 lds_addr(const void* p) { return (u32)(uintptr_t)(const LAS char*)p; }
__device__ __forceinline__ void flag_set(u32 a, u32 v) {
  asm volatile("ds_write_b32 %0, %1" ::"v"(a), "v"(v) : "memory");
}
template <int SLEEP, u32 V>
__device__ __forceinline__ void flag_wait_(u32 a) {
  u32 f;
  if (SLEEP)
    asm volatile("s_branch .Lfr%=\n.Lfw%=:\n\ts_sleep %3\n.Lfr%=:\n\tds_read_b32 %0, %1\n\ts_waitcnt lgkmcnt(0)\n\tv_cmp_gt_u32 vcc, %2, %0\n\ts_cbranch_vccnz .Lfw%="
                 : "=&v"(f) : "v"(a), "n"(V), "n"(SLEEP) : "vcc", "memory");
  else
    asm volatile(".Lfw%=:\n\tds_read_b32 %0, %1\n\ts_waitcnt lgkmcnt(0)\n\tv_cmp_gt_u32 vcc, %2, %0\n\ts_cbranch_vccnz .Lfw%="
                 : "=&v"(f) : "v"(a), "n"(V) : "vcc", "memory");
}
#define FWAIT(sl, a, v) flag_wait_<sl, (u32)(v)>(a)
__device__ __forceinline__ bool flag_peek(u32 a, u32 v) {
  u32 f;
  asm volatile("ds_read_b32 %0, %1\n\ts_waitcnt lgkmcnt(0)" : "=v"(f) : "v"(a) : "memory");
  return __all(f >= v);
}
template <int N, int AUX = 16, typename RS>
__device__ __forceinline__ int poll(u32x4 (&v)[N], RS rs, u32 off, u32 step, u32 t0, u32 t1) {
  int spin = 0;
#pragma nounroll
  for (; spin < SPIN_CAP; ++spin) {
    bool bad[N], any = false;
#pragma unroll
    for (int i = 0; i < N; ++i) { bad[i] = tbad(tdiff(v[i], t0, t1)); any |= bad[i]; }
    if (!__any(any)) break;
    PIN();
    if (PSLEEP) __builtin_amdgcn_s_sleep(PSLEEP);
#pragma unroll
    for (int i = 0; i < N; ++i)
      if (bad[i]) v[i] = __builtin_amdgcn_raw_buffer_load_b128(rs, off + (u32)i * step, 0, AUX);
  }
  return spin;
}

#define RELOAD(W, v, m, off, imm)                                                                              \
  asm volatile("s_mov_b64 exec, %1\n\ts_cbranch_execz .Lrl%=\n\tglobal_load_dwordx" #W " %0, %2, %3 offset:%4 sc1\n.Lrl%=:\n\ts_mov_b64 exec, -1" \
               : "+v"(v) : "s"(m), "v"(off), "s"(ws), "n"(imm) : "memory")
#define LOADW(W, v, off, imm) asm volatile("global_load_dwordx" #W " %0, %1, %2 offset:%3 sc1" : "=v"(v) : "v"(off), "s"(ws), "n"(imm) : "memory")
#define CAPN(m, c) "s_sub_u32 %0, %0, 1\n\ts_cselect_b32 %1, 0, " m "\n\ts_waitcnt vmcnt(" c ")"
#define CAPW(m) CAPN(m, "0")
#define STR_(x) #x
#define STR(x) STR_(x)
#define LOADA(W, v, off, imm) asm volatile("global_load_dwordx" #W " %0, %1, %2 offset:%3 sc1" : "+v"(v) : "v"(off), "s"(ws), "n"(imm) : "memory")
#define RELOAD1(W, v, m, off, imm)                                                                               \
  asm volatile("s_or_b64 exec, %1, 1\n\tglobal_load_dwordx" #W " %0, %2, %3 offset:%4 sc1\n\ts_mov_b64 exec, -1" \
               : "+v"(v) : "s"(m), "v"(off), "s"(ws), "n"(imm) : "scc", "memory")
__device__ __forceinline__ u32 or_and(u32 a, u32 b, u32 c) { return __builtin_amdgcn_bitop3_b32(a, b, c, 0xA8); }
__device__ __forceinline__ u32x4 untag(const u32x4& v, u32 t0, u32 t1) { return u32x4{v[0] ^ t0, v[1] ^ t1, v[2] ^ t0, v[3] ^ t1}; }
__device__ __forceinline__ unsigned long long lanes_set(u32 d) { return __builtin_amdgcn_uicmp(d, 0u, 33); }

#define NOW(i) do { } while (0)

#define YNOW(i) NOW(i)
#define XNOW(i) do { } while (0)
constexpr int Q_LD = 144;
constexpr int XN_LD = 520;
constexpr int OA_LD = 72;
constexpr int S32 = 96;
constexpr int S64 = 160;
constexpr int S128 = 288;

__global__ void __launch_bounds__(768, 3)
fused_kernel(const u16* __restrict__ x, const u16* __restrict__ wq, const u16* __restrict__ w1, char* ws,
             const u16* __restrict__ kc, const u16* __restrict__ vc, const u16* __restrict__ wo,
             const u16* __restrict__ w2_, const u16* __restrict__ b1_, const u16* __restrict__ b2_, u16* out_) {
  const u16* const w2 = w2_; const u16* const b1 = b1_; const u16* const b2 = b2_; u16* const out = out_;
  __shared__ __attribute__((aligned(16))) char s_wa[384 * S32];
  __shared__ __attribute__((aligned(16))) char s_wo[64 * S128];
  __shared__ __attribute__((aligned(16))) char s_w2[64 * S64];
  __shared__ __attribute__((aligned(16))) u16 s_xn[8 * XN_LD];
  __shared__ __attribute__((aligned(16))) u16 s_q[4 * Q_LD];
  __shared__ __attribute__((aligned(16))) u16 s_oA[8 * OA_LD];
  __shared__ __attribute__((aligned(16))) u16 s_hA[8 * OA_LD];
  __shared__ __attribute__((aligned(16))) float s_ml[256 + 128];
  __shared__ __attribute__((aligned(16))) float s_o[8 * 512];
  __shared__ __attribute__((aligned(16))) float s_g[64];
  __shared__ __attribute__((aligned(16))) u32 s_flag[64];
  __shared__ __attribute__((aligned(8))) u32 s_tag2[2];
#define s_tag s_tag2[0]

  NOW(0);
  const unsigned long long tg64 = __builtin_amdgcn_s_memrealtime();
  const u32 tg0 = (u32)tg64;
  u32 cpf;
  {
    unsigned long long pc0;
    const u32 ln = min(((threadIdx.x >> 6) - 4u) * (u32)CPF + (threadIdx.x & 63u) % (u32)CPF, (u32)CPFL - 1u) * 128u;
    asm volatile("v_cmp_lt_u32 vcc, 0xff, %3\n\ts_cbranch_vccz .Lcpf%=\n\ts_getpc_b64 %1\n\ts_andn2_b64 %1, %1, 0xff\n\t"
                 "global_load_dword %0, %2, %1\n.Lcpf%=:"
                 : "=&v"(cpf), "=&s"(pc0) : "v"(ln), "v"(threadIdx.x) : "vcc", "scc", "memory");
  }
  const int bid = blockIdx.x, tid = threadIdx.x, lane = tid & 63;
  const int wave = __builtin_amdgcn_readfirstlane(tid >> 6);
  const int kvh = bid & 7, r = bid >> 3;
  const int ab = (r >> 2) ^ (ABX), aj = r & 3;
  const bool ffn = bid < 224;
  const int kvq = kvh ^ (QO), yb = kvh ^ (YX);
  const int kvo = ((kvh + 3) & 3) + 4 * (aj >> 1), oj = 2 * (kvh >> 2) + (aj & 1);
  const u32 foob = ffn ? 0u : OOB;
  const int n = lane & 15, g = lane >> 4;
  const u32 fS = lds_addr(&s_flag[8 + (lane & 7)]);
  const u32 fQ = lds_addr(&s_flag[lane & 1]);
  const u32 fme = lds_addr(&s_flag[wave + (wave < 4 ? 0 : 4)]);
  auto rs_w = __builtin_amdgcn_make_buffer_rsrc((void*)ws, 0, WS_END, 0x00020000);

  u32x4 xa, xb, Wt[8];
  u32 epoch;
  {
    u32 xo = (u32)((2 * (wave & 3) + (lane >> 5)) * DM + (lane & 31) * 8) * 2u;
    u32 o0 = (u32)((256 * (r & 1) + 64 * (wave & 3) + (lane >> 2)) * (HQ * DH) + kvq * 512 + 32 * QHACK((r >> 1) ^ QX) + 8 * (lane & 3)) * 2u;
    if (WFL & 1) o0 = (u32)((128 * (r & 3) + 32 * (wave & 3) + (lane >> 3)) * (HQ * DH) + kvq * 512 + 64 * ((r >> 2) ^ WQX) + 8 * (lane & 7)) * 2u;
    constexpr u32 OS_ = (WFL & 1) ? 0x10000u : 0x20000u;
    u32 o1 = o0 + OS_, o2 = o0 + 2 * OS_, o3 = o0 + 3 * OS_, z = 0u;
    u32 fz = lds_addr(&s_flag[lane]), fv = lane >= 60 ? 0xFF800000u : 0u;
    const int sw = wave - 4;
    u32 k0 = (u32)(((ab * HKV + kvh) * SK + (SK - WIN) + aj * 256) * DH) * 2u +
             (u32)(sw * 8192 + ((KV2 ? (n & 12) + ((n >> 1) & 1) : n >> 1) ^ ((XR) & 31)) * 256 + (n & 1) * 64 + g * 16);
    u32 k2 = k0 ^ (KV2 ? 512u : 2048u);
    asm volatile("" : "+v"(xo), "+v"(o0), "+v"(o1), "+v"(o2), "+v"(o3), "+v"(z), "+v"(fz), "+v"(fv), "+v"(k0), "+v"(k2));
    asm volatile("s_cmp_gt_u32 %[wv], 3\n\ts_cbranch_scc1 .Lps%=\n\t"
                 "global_load_dwordx4 %[xa], %[xo], %[x]\n\t"
                 "global_load_dwordx4 %[xb], %[xo], %[x] offset:512\n\t"
                 "global_load_dword %[ep], %[z], %[ws] offset:%[eo]" ESC "\n\t"
                 "global_load_dwordx4 %[w0], %[o0], %[wq] nt\n\t"
                 "global_load_dwordx4 %[w1], %[o1], %[wq] nt\n\t"
                 "global_load_dwordx4 %[w2], %[o2], %[wq] nt\n\t"
                 "global_load_dwordx4 %[w3], %[o3], %[wq] nt\n\t"
                 "s_barrier\n\ts_setprio 3\n\ts_branch .Lpe%=\n"
                 ".Lps%=:\n\tds_write_b32 %[fz], %[fv]\n\t"
                 ".if %[kf] == 0\n\t" PLW "s_barrier\n\t.endif\n\t"
                 "global_load_dwordx4 %[w0], %[k0], %[kc] nt\n\t"
                 "global_load_dwordx4 %[w1], %[k0], %[kc] offset:128 nt\n\t"
                 ".if %[kf] == 2\n\t" PLW "s_barrier\n\t.endif\n\t"
                 "global_load_dwordx4 %[w2], %[k2], %[kc] nt\n\t"
                 "global_load_dwordx4 %[w3], %[k2], %[kc] offset:128 nt\n\t"
                 ".if %[kf] == 4\n\t" PLW "s_barrier\n\t.endif\n"
                 ".Lpe%=:"
                 : [xa] "=&v"(xa), [xb] "=&v"(xb), [ep] "=&v"(epoch), [w0] "=&v"(Wt[0]), [w1] "=&v"(Wt[1]), [w2] "=&v"(Wt[2]), [w3] "=&v"(Wt[3])
                 : [wv] "s"(wave), [xo] "v"(xo), [x] "s"(x), [z] "v"(z), [ws] "s"(ws), [wq] "s"(wq), [fz] "v"(fz), [fv] "v"(fv),
                   [o0] "v"(o0), [o1] "v"(o1), [o2] "v"(o2), [o3] "v"(o3), [k0] "v"(k0), [k2] "v"(k2), [kc] "s"(kc), [eo] "n"(EOFF), [kf] "n"(KFIRST)
                 : "scc", "memory");
  }
  asm volatile("" ::"s"(w1), "s"(vc), "s"(wo), "s"(w2), "s"(b1), "s"(b2), "s"(out), "s"(tg64));
  if (wave < 4) {
    const int fw = wave, f1 = wave & 1;
    const bool isq = wave < 2;
    const u32 tstart = (u32)__builtin_amdgcn_s_memrealtime();
    XNOW(2);
    auto rs_x = __builtin_amdgcn_make_buffer_rsrc((void*)x, 0, NB * DM * 2, 0x00020000);
    auto rs_q = __builtin_amdgcn_make_buffer_rsrc((void*)wq, 0, DM * HQ * DH * 2, 0x00020000);
    auto rs_a = __builtin_amdgcn_make_buffer_rsrc((void*)w1, 0, DM * FF * 2, 0x00020000);
    const u32 fF = lds_addr(&s_flag[lane & 3]);
    const u32 fP = lds_addr(&s_flag[(wave & 2) + (lane & 1)]);
    const int combo = (r >> 1) * 8 + kvh;
    const int cg56 = 2 * (combo >> 2) + (r & 1), ks4 = combo & 3;
    const int krow0 = isq ? 256 * (r & 1) : 128 * ks4;
    const int qc = (r >> 1) ^ QX;

    const u32 w1o = (isq ? OOB : foob) + ((WFL & 2) ? (u32)((64 * kvh + 32 * f1 + (lane >> 3)) * FF + 64 * r + 8 * (lane & 7)) * 2u
                                                    : (u32)((krow0 + 64 * f1 + (lane >> 2)) * FF + 32 * cg56 + 8 * (lane & 3)) * 2u);
    constexpr int W1S = (WFL & 2) ? 8 : 16;
    if (W1E == 1) {
#pragma unroll
      for (int i = 0; i < 4; ++i) Wt[4 + i] = LD_NT(rs_a, w1o + (u32)(W1S * i * FF) * 2u);
    }
    asm volatile("s_waitcnt vmcnt(%2)" : "+v"(xa), "+v"(xb) : "n"(W1E == 1 ? 9 : 5) : "memory");
    if (W1E == 2) {
#pragma unroll
      for (int i = 0; i < 4; ++i) Wt[4 + i] = LD_NT(rs_a, w1o + (u32)(W1S * i * FF) * 2u);
    }
    asm volatile("" : "+v"(xa), "+v"(xb)::"memory");
    NOW(1);
    {
      float v[16];
#pragma unroll
      for (int i = 0; i < 4; ++i) { v[2 * i] = bf_lo(xa[i]); v[2 * i + 1] = bf_hi(xa[i]); v[8 + 2 * i] = bf_lo(xb[i]); v[9 + 2 * i] = bf_hi(xb[i]); }
      float s = (((v[0] + v[1]) + (v[2] + v[3])) + ((v[4] + v[5]) + (v[6] + v[7]))) + (((v[8] + v[9]) + (v[10] + v[11])) + ((v[12] + v[13]) + (v[14] + v[15])));
      float mean = half_sum(s) * (1.f / DM);
      float q0 = 0.f, q1 = 0.f;
#pragma unroll
      for (int i = 0; i < 8; ++i) { float d = v[i] - mean; q0 += d * d; float e = v[8 + i] - mean; q1 += e * e; }
      float var = rbf(half_sum(q0 + q1) * (1.f / DM));
      float mb = rbf(mean);
      float den = rbf(sqrtf(rbf(var + 1e-5f)));
      float inv = 1.f / den;
      u32x4 oa, ob;
#pragma unroll
      for (int i = 0; i < 4; ++i) {
        u32 d = pack_bf(v[2 * i] - mb, v[2 * i + 1] - mb);
        oa[i] = pack_bf(bf_lo(d) * inv, bf_hi(d) * inv);
        u32 e = pack_bf(v[8 + 2 * i] - mb, v[9 + 2 * i] - mb);
        ob[i] = pack_bf(bf_lo(e) * inv, bf_hi(e) * inv);
      }
      u16* p = &s_xn[(2 * fw + (lane >> 5)) * XN_LD + (lane & 31) * 8];
      *(u32x4*)p = oa;
      *(u32x4*)(p + 256) = ob;
    }
    if (W1E == 3) {
      PIN();
#pragma unroll
      for (int i = 0; i < 4; ++i) Wt[4 + i] = LD_NT(rs_a, w1o + (u32)(W1S * i * FF) * 2u);
    }
    asm volatile("s_waitcnt vmcnt(%1)" : "+v"(epoch) : "n"(W1E && W1E < 4 ? 8 : 4) : "memory");
    const u32 tag = __builtin_amdgcn_readfirstlane(epoch) + 0x9E3779B9u;
    const u32 t0 = tag & 0x7FFu, t1 = (tag >> 11) & 0x7FFu;
    YNOW(2);
    char* const wbase = s_wa + (isq ? 0 : 256 * S32);
    {
      char* p = s_wa + (64 * fw + (lane >> 2)) * S32 + (lane & 3) * 16;
#pragma unroll
      for (int i = 0; i < 4; ++i) {
        asm volatile("s_waitcnt vmcnt(%1)" : "+v"(Wt[i]) : "n"((W1E && W1E < 4 ? 7 : 3) - i) : "memory");
        *(u32x4*)(p + 16 * i * S32) = Wt[i];
      }
    }
    if (tid == 0) { s_tag = tag; if (OLATE || SLATE) s_tag2[1] = tstart; }
    flag_set(fme, 1u);
    if (W1E == 4 && !isq) {
#pragma unroll
      for (int i = 0; i < 4; ++i) Wt[4 + i] = LD_NT(rs_a, w1o + (u32)(W1S * i * FF) * 2u);
      PIN();
    }
    FWAIT(0, fF, 1u);
    YNOW(3);
    auto rs_b = __builtin_amdgcn_make_buffer_rsrc((void*)(isq ? wo : w2), 0, isq ? DM * HQ * DH * 2 : DM * FF * 2, 0x00020000);
    auto rs_b1 = __builtin_amdgcn_make_buffer_rsrc((void*)b1, 0, FF * 2, 0x00020000);
    auto rs_b2 = __builtin_amdgcn_make_buffer_rsrc((void*)b2, 0, DM * 2, 0x00020000);
    const char* wp = wbase + f1 * 32 + (8 * g + (n >> 2)) * S32 + (n & 3) * 8;
    const u16* xp = &s_xn[(n & 7) * XN_LD + krow0 + 8 * g];
    const f32x4 Z = {0.f, 0.f, 0.f, 0.f};
    if (isq) {
      f32x4 D0 = Z, D1 = Z;
#pragma unroll
      for (int k = 0; k < 4; ++k) {
        D0 = mfma(tr_read(wp + 64 * k * S32, S32), *(const u32x4*)(xp + 64 * k), D0);
        D1 = mfma(tr_read(wp + (64 * k + 32) * S32, S32), *(const u32x4*)(xp + 64 * k + 32), D1);
      }
      D0 += D1;
      const u32 qtag = tag;
      const u32x4 qd = {asu(n < 8 ? D0[0] : D0[2]), qtag, asu(n < 8 ? D0[1] : D0[3]), qtag};
      const u32 qo = QP_OFF + (u32)((kvq * 8 + (n & 7)) * 8192 + (r & 1) * 4096 + (32 * qc + 16 * f1 + 4 * g + 2 * (n >> 3)) * 8);
      ST_SC(qd, rs_w, qo);
      PIN();
      YNOW(4);
      u32 qoff = QP_OFF + (u32)((kvh * 8 + ab) * 8192 + f1 * 2048 + lane * 16);
      u32 qoff2 = qoff + 4096u, qw = lds_addr(&s_q[f1 * 2 * Q_LD + lane * 2]), fa = fme, two = 2u;
      asm volatile("" : "+v"(qoff), "+v"(qoff2), "+v"(qw), "+v"(fa), "+v"(two));
#pragma nounroll
      for (int spin = 0; spin < SPIN_CAP; ++spin) {
        if ((u32)__builtin_amdgcn_s_memrealtime() - tstart >= (u32)QT) break;
        __builtin_amdgcn_s_sleep(1);
      }
      NOW(6);
      u32x4 qv[4];
      LOADW(4, qv[0], qoff, 0); LOADW(4, qv[1], qoff, 1024); LOADW(4, qv[2], qoff2, 0); LOADW(4, qv[3], qoff2, 1024);
      u32 qa, qb;
      {
        u32 d[4] = {0u, 0u, 0u, 0u};
        unsigned long long bad;
        int spin = SPIN_CAP;
#pragma nounroll
        do {
          RELOAD(4, qv[0], lanes_set(d[0]), qoff, 0);
          RELOAD(4, qv[1], lanes_set(d[1]), qoff, 1024);
          RELOAD(4, qv[2], lanes_set(d[2]), qoff2, 0);
          RELOAD(4, qv[3], lanes_set(d[3]), qoff2, 1024);
          u32 msk;
          asm volatile(CAPW("-1") : "+s"(spin), "=s"(msk), "+v"(qv[0]), "+v"(qv[1]), "+v"(qv[2]), "+v"(qv[3])::"scc", "memory");
#pragma unroll
          for (int i = 0; i < 4; ++i) d[i] = __builtin_amdgcn_bitop3_b32(qv[i][1], qv[i][3], tag, 0x7E);
          bad = lanes_set(or_and(d[0] | d[1] | d[2], d[3], msk));
          asm volatile("" : "+s"(bad));
          qa = pack_bf(asf(qv[0][0]) + asf(qv[2][0]), asf(qv[0][2]) + asf(qv[2][2]));
          qb = pack_bf(asf(qv[1][0]) + asf(qv[3][0]), asf(qv[1][2]) + asf(qv[3][2]));
          asm volatile("" : "+v"(qa), "+v"(qb));
        } while (bad != 0ull);
      }
      asm volatile("ds_write2_b32 %0, %1, %2 offset1:%3\n\tds_write_b32 %4, %5" ::"v"(qw), "v"(qa), "v"(qb), "n"(Q_LD / 2), "v"(fa), "v"(two) : "memory");
      __builtin_amdgcn_sched_barrier(0);
      NOW(5);
      const int rid = YLOC ? 32 * (kvh ^ (RX)) + r : SFULL ? ((bid & 7) + 8 * (bid >> 4)) * 2 + ((bid >> 3) & 1) : bid;
      const u32 ocol = (u32)(4 * (rid >> 1) + (lane & 3));
      const u32 oidx = (u32)((4 * (rid & 1) + g) * DM) + ocol;
      const u32 xb2 = f1 ? __builtin_amdgcn_raw_buffer_load_b16(rs_b2, ocol * 2u, 0, 0)
                         : __builtin_amdgcn_raw_buffer_load_b16(rs_x, oidx * 2u, 0, 0);
      const u32 woo = (u32)((kvo * 512 + 64 * ab + 32 * f1 + g) * DM + 128 * oj + 8 * n) * 2u;
      PIN();
      auto stats = [&]() {
        const f32x4* const mp = (const f32x4*)&s_ml[((lane & 3) * 8 + ((lane >> 2) & 7)) * 8];
        const f32x4 u = mp[0], v = mp[1];
        const float m = asf(s_flag[60 + (lane & 3)]);
        float l = ((u[0] + u[2]) + (v[0] + v[2])) * __expf(u[1] - m);
        l += dpp<0x124>(l); l += dpp<0x128>(l);
        l = xor16_add(l);
        const u32 spo = lane < 16 ? OS_OFF + (u32)(((kvh * 8 + ab) * 4 + aj) * 128 + lane * 8) : OOB;
        __builtin_amdgcn_raw_buffer_store_b64(u32x2{enc(m, t0), enc(l, t1)}, rs_w, spo, 0, 16);
        PIN();
      };
      bool std_ = !f1;
#pragma nounroll
      for (int spin = 0; spin < (SPIN_CAP << 4); ++spin) {
        if (WOA) {
          if (flag_peek(fS, WOP) && (!WTIME || (u32)__builtin_amdgcn_s_memrealtime() - tstart >= (u32)WTIME)) { __builtin_amdgcn_s_sleep(WOA - 1); break; }
          if (WMAX && flag_peek(fS + 32u, 1u) && (u32)__builtin_amdgcn_s_memrealtime() - tstart >= (u32)WMAX) break;
        } else
        if (flag_peek(fS + 32u, 1u) && (!WTIME || (u32)__builtin_amdgcn_s_memrealtime() - tstart >= (u32)WTIME)) break;
        if (!std_ && flag_peek(fS, 2u)) { stats(); std_ = true; }
        __builtin_amdgcn_s_sleep(1);
      }
      NOW(7);
#pragma unroll
      for (int i = 0; i < 8; ++i) Wt[i] = LD_NT(rs_b, woo + (u32)(4 * i * DM) * 2u);
      PIN();
      if (!std_) { FWAIT(1, fS, 2u); stats(); }
      NOW(8);
      {
        char* p = s_wo + (32 * f1 + g) * S128 + n * 16;
#pragma unroll
        for (int i = 0; i < 8; ++i) *(u32x4*)(p + 4 * i * S128) = Wt[i];
      }
      flag_set(fme, 3u);
      NOW(9);
      if (f1) {
        const u32 yoff = YP_OFF + (u32)(rid * 2048 + lane * 16);
        if (YT) { FWAIT(1, lds_addr(&s_flag[2 + (lane & 1)]), 4u); FWAIT(1, fS, YT); }
        u32x4 v[2] = {LD_SC(rs_w, yoff), LD_SC(rs_w, yoff + 1024u)};
        if (lane >= 48) v[1] = u32x4{t0, t1, t0, t1};
        poll<2>(v, rs_w, yoff, 1024u, t0, t1);
        NOW(10);
        float y = rows_scatter(dec(v[0][0]) + dec(v[1][0]), dec(v[0][1]) + dec(v[1][1]), dec(v[0][2]) + dec(v[1][2]), dec(v[0][3]) + dec(v[1][3]));
        y += dpp<0x124>(y); y += dpp<0x128>(y);
        y = rbf(rbf(y) + bf_lo(xb2));
        s_g[lane] = rbf(y * __builtin_amdgcn_rcpf(1.f + __expf(-y)));
        flag_set(fme, 4u);
        NOW(11);
      } else {
        constexpr u32 SST = 2048u;
        u32 soff = SP_OFF + (u32)((rid >> 1) * 8192 + (lane >> 2) * 128 + (rid & 1) * 64 + (lane & 3) * 16), soff2 = soff + 2u * SST;
        u32 oo = n < 4 ? oidx * 2u : OOB, fg = lds_addr(&s_flag[1]), ga = lds_addr(&s_g[lane]);
        float xf = bf_lo(xb2), gt;
        auto rs_o = __builtin_amdgcn_make_buffer_rsrc((void*)out, 0, NB * DM * 2, 0x00020000);
        asm volatile("" : "+v"(soff), "+v"(soff2), "+v"(oo), "+v"(fg), "+v"(ga), "+v"(xf));
        __builtin_amdgcn_s_waitcnt(0x0F70);
        FWAIT(FSL, fS, 7u);
        NOW(10);
        u32x4 v[4];
        LOADW(4, v[0], soff, 0); LOADW(4, v[1], soff, SST); LOADW(4, v[2], soff2, 0); LOADW(4, v[3], soff2, SST);
        {
          u32 fl;
          asm volatile(".Lgw%=:\n\tds_read_b32 %0, %2\n\tds_read_b32 %1, %3\n\ts_waitcnt lgkmcnt(0)\n\tv_cmp_gt_u32 vcc, 4, %0\n\ts_cbranch_vccnz .Lgw%="
                       : "=&v"(fl), "=&v"(gt) : "v"(fg), "v"(ga) : "vcc", "memory");
        }
        float z0, z1;
        constexpr u32 d0 = SNP > 1 ? 1u : 0u;
        u32 d[4] = {d0, d0, d0, d0};
        unsigned long long bad;
        int spin = SPIN_CAP;
#pragma unroll
        for (int k = 1; k < SNP; ++k) {
          __builtin_amdgcn_s_sleep(SPD);
          if (k + 1 < SNP) { LOADA(4, v[0], soff, 0); LOADA(4, v[1], soff, SST); LOADA(4, v[2], soff2, 0); LOADA(4, v[3], soff2, SST); }
        }
#pragma nounroll
        do {
          if (SNP > 1) {
            RELOAD1(4, v[0], lanes_set(d[0] & 0x7FFu), soff, 0);
            RELOAD1(4, v[1], lanes_set(d[1] & 0x7FFu), soff, SST);
            RELOAD1(4, v[2], lanes_set(d[2] & 0x7FFu), soff2, 0);
            RELOAD1(4, v[3], lanes_set(d[3] & 0x7FFu), soff2, SST);
          } else {
            RELOAD(4, v[0], lanes_set(d[0] & 0x7FFu), soff, 0);
            RELOAD(4, v[1], lanes_set(d[1] & 0x7FFu), soff, SST);
            RELOAD(4, v[2], lanes_set(d[2] & 0x7FFu), soff2, 0);
            RELOAD(4, v[3], lanes_set(d[3] & 0x7FFu), soff2, SST);
          }
          u32 msk;
          asm volatile(CAPN("0x7ff", STR(4 * (SNP - 1))) : "+s"(spin), "=s"(msk), "+v"(v[0]), "+v"(v[1]), "+v"(v[2]), "+v"(v[3])::"scc", "memory");
          u32x4 y[4];
#pragma unroll
          for (int i = 0; i < 4; ++i) {
            y[i] = untag(v[i], t0, t1);
            asm volatile("" : "+v"(y[i]));
            d[i] = (y[i][0] | y[i][1] | y[i][2]) | y[i][3];
          }
          bad = lanes_set(or_and(d[0] | d[1] | d[2], d[3], msk));
          asm volatile("" : "+s"(bad));
          const f32x4 a = (__builtin_bit_cast(f32x4, y[0]) + __builtin_bit_cast(f32x4, y[1])) + (__builtin_bit_cast(f32x4, y[2]) + __builtin_bit_cast(f32x4, y[3]));
          auto p = __builtin_amdgcn_permlane32_swap(asu(a[0]), asu(a[2]), false, false);
          auto q = __builtin_amdgcn_permlane32_swap(asu(a[1]), asu(a[3]), false, false);
          z0 = asf(p[0]) + asf(p[1]); z1 = asf(q[0]) + asf(q[1]);
          asm volatile("" : "+v"(z0), "+v"(z1));
        } while (bad != 0ull);
        NOW(11);
        auto p = __builtin_amdgcn_permlane16_swap(asu(z0), asu(z1), false, false);
        float sv = asf(p[0]) + asf(p[1]);
        sv += dpp<0x124>(sv); sv += dpp<0x128>(sv);
        const float o = xf + rbf(rbf(sv) * gt);
        __builtin_amdgcn_raw_buffer_store_b16((u16)pack_bf(o, o), rs_o, oo, 0, 0);
        if (bid == 0 && tid == 0) __builtin_amdgcn_raw_buffer_store_b32(tag, rs_w, EOFF, 0, 16);
        if (SNP > 1) asm volatile("s_waitcnt vmcnt(0)" ::"v"(v[0]), "v"(v[1]), "v"(v[2]), "v"(v[3]) : "memory");
      }
    } else if (FEXIT && !ffn) {
      flag_set(fme, 4u);
    } else {
      const u32 w2o = foob + (u32)((64 * r + 32 * f1 + (lane >> 3)) * DM + 64 * yb + 8 * (lane & 7)) * 2u;
      const int hc = 32 * f1 + 16 * (g >> 1) + n;
      const u32 b1v = __builtin_amdgcn_raw_buffer_load_b16(rs_b1, foob + (u32)(64 * r + hc) * 2u, 0, 0);
      if (W1T) FWAIT(1, fS, W1T);
      NOW(7);
      {
        const u32 o = foob + (u32)((krow0 + 64 * f1 + (lane >> 2)) * FF + 32 * cg56 + 8 * (lane & 3)) * 2u;
#pragma unroll
        for (int i = 0; i < 4; ++i) Wt[i] = W1E ? Wt[4 + i] : LD_NT(rs_a, o + (u32)(16 * i * FF) * 2u);
        PIN();
        if (W2T <= W1T) {
#pragma unroll
          for (int i = 0; i < 4; ++i) Wt[4 + i] = LD_NT(rs_b, w2o + (u32)(8 * i * DM) * 2u);
          PIN();
        }
        char* p = wbase + (64 * f1 + (lane >> 2)) * S32 + (lane & 3) * 16;
#pragma unroll
        for (int i = 0; i < 4; ++i) *(u32x4*)(p + 16 * i * S32) = Wt[i];
      }
      flag_set(fme, 2u);
      FWAIT(0, fP, 2u);
      NOW(8);
      f32x4 D0 = Z, D1 = Z;
#pragma unroll
      for (int k = 0; k < 2; ++k) {
        D0 = mfma(*(const u32x4*)(xp + 64 * k), tr_read(wp + 64 * k * S32, S32), D0);
        D1 = mfma(*(const u32x4*)(xp + 64 * k + 32), tr_read(wp + (64 * k + 32) * S32, S32), D1);
      }
      D0 += D1;
      ST_SC(enc4(D0, t0, t1), rs_w, g < 2 ? foob + HP_OFF + (u32)((cg56 * 4 + ks4) * 1024 + f1 * 512 + lane * 16) : (WFL & 8) ? foob + WS_END - 256 * KB + (u32)((cg56 * 4 + ks4) * 1024 + f1 * 512 + (lane & 31) * 16) : OOB);
      PIN();
      YNOW(4);
      if (W2T > W1T && W1T) {
        FWAIT(1, fS, W2T);
#pragma unroll
        for (int i = 0; i < 4; ++i) Wt[4 + i] = LD_NT(rs_b, w2o + (u32)(8 * i * DM) * 2u);
        PIN();
        WAITV(4);
      } else {
        WAITV(0);
      }
      NOW(6);
      if (W2T == 11) {
#pragma unroll
        for (int i = 0; i < 4; ++i) Wt[4 + i] = LD_NT(rs_b, w2o + (u32)(8 * i * DM) * 2u);
        PIN();
      }
      if (HT) {
#pragma nounroll
        for (int spin = 0; spin < SPIN_CAP; ++spin) {
          if ((u32)__builtin_amdgcn_s_memrealtime() - tstart >= (u32)HT) break;
          __builtin_amdgcn_s_sleep(1);
        }
      } else if (HDELAY) __builtin_amdgcn_s_sleep(HDELAY);
      const u32 hoff = foob + HP_OFF + (u32)((2 * r + f1) * 4096 + lane * 16);
      u32x4 hv[4];
#pragma unroll
      for (int i = 0; i < 4; ++i) hv[i] = (i >= HQ1) ? u32x4{t0, t1, t0, t1} : LD_SC(rs_w, hoff + (u32)i * 1024u);
      if (W2T == 10) {
        PIN();
#pragma unroll
        for (int i = 0; i < 4; ++i) Wt[4 + i] = LD_NT(rs_b, w2o + (u32)(8 * i * DM) * 2u);
        PIN();
      }
      if (WFL & 8) {
        const u32 q2 = hoff - HP_OFF + WS_END - 256 * KB;
        u32x4 e0 = LD_SC(rs_w, q2), e1 = LD_SC(rs_w, q2 + 1024u), e2 = LD_SC(rs_w, q2 + 2048u), e3 = LD_SC(rs_w, q2 + 3072u);
        asm volatile("" ::"v"(e0), "v"(e1), "v"(e2), "v"(e3));
      }
      if (ffn) poll<4>(hv, rs_w, hoff, 1024u, t0, t1);
      NOW(5);
#pragma unroll
      for (int c = 0; c < 4; ++c) {
        float v = (dec(hv[0][c]) + dec(hv[1][c])) + (dec(hv[2][c]) + dec(hv[3][c]));
        float t = rbf(rbf(v) + bf_lo(b1v));
        float u = 0.7978845608028654f * (t + 0.044715f * t * t * t);
        float th = 1.f - 2.f * __builtin_amdgcn_rcpf(__expf(2.f * u) + 1.f);
        float hvv = 0.5f * t * (1.f + th);
        s_hA[(4 * (g & 1) + c) * OA_LD + hc] = (u16)pack_bf(hvv, hvv);
      }
      if (W2T > W1T && !W1T && W2T < 10) {
        if (W2T == 9) FWAIT(1, fS + 32u, 1u); else if (W2T < 8) FWAIT(1, fS, W2T);
        if (W2TIME) {
#pragma nounroll
          for (int spin = 0; spin < SPIN_CAP; ++spin) {
            if ((u32)__builtin_amdgcn_s_memrealtime() - tstart >= (u32)W2TIME) break;
            __builtin_amdgcn_s_sleep(1);
          }
        }
        NOW(7);
#pragma unroll
        for (int i = 0; i < 4; ++i) Wt[4 + i] = LD_NT(rs_b, w2o + (u32)(8 * i * DM) * 2u);
        PIN();
      }
      {
        char* p = s_w2 + (32 * f1 + (lane >> 3)) * S64 + (lane & 7) * 16;
#pragma unroll
        for (int i = 0; i < 4; ++i) *(u32x4*)(p + 8 * i * S64) = Wt[4 + i];
      }
      flag_set(fme, 3u);
      FWAIT(0, fP, 3u);
      NOW(9);
      {
        const u16* hp = &s_hA[(n & 7) * OA_LD + 8 * g];
        const u32x4 h0 = *(const u32x4*)hp, h1 = *(const u32x4*)(hp + 32);
        const char* bp = s_w2 + f1 * 64 + (8 * g + (n >> 2)) * S64 + (n & 3) * 8;
        D0 = mfma(h0, tr_read(bp, S64), Z);
        D1 = mfma(h0, tr_read(bp + 32, S64), Z);
        D0 = mfma(h1, tr_read(bp + 32 * S64, S64), D0);
        D1 = mfma(h1, tr_read(bp + 32 * S64 + 32, S64), D1);
        const int col = 64 * yb + 32 * f1 + 16 * (g >> 1) + n;
        const u32x4 ye = enc4(g < 2 ? D0 : D1, t0, t1);
        const u32 yo = foob + YP_OFF + (u32)(((col >> 2) * 2 + (g & 1)) * 2048 + r * 64 + (n & 3) * 16);
        if (YST) { asm volatile("" ::"v"(ye), "v"(yo)); FWAIT(1, fS, YST); }
        ST_SC(ye, rs_w, yo);
        PIN();
        NOW(10);
        if (YT) { WAITV(0); flag_set(fme, 4u); }
      }
      NOW(11);
    }
  } else {
    const int sw = wave - 4;
    u32 to2 = 0, to3 = 0;
    const u32 tkv = (u32)(((sw * HKV + kvh) * SK + (SK - WIN)) * DH) * 2u;
    const u32 kvbase = (u32)(((ab * HKV + kvh) * SK + (SK - WIN) + aj * 256) * DH) * 2u;
    const int xr = (XR) & 31;
    u32 koffs[8], voffs[8];
#pragma unroll
    for (int i = 0; i < 8; ++i) {
      const int s = i >> 1;
      const int kr = KV2 ? 16 * (s >> 1) + (n & 12) + 2 * (s & 1) + ((n >> 1) & 1) : 8 * s + (n >> 1);
      const int vr = KV2 ? 16 * (i & 1) + 4 * g + (i >> 1) : 16 * (i & 1) + 8 * (i >> 2) + 2 * g + ((i >> 1) & 1);
      koffs[i] = kvbase + (u32)(sw * 8192 + (kr ^ xr) * 256 + (i & 1) * 128 + (n & 1) * 64 + g * 16);
      voffs[i] = kvbase + (u32)(sw * 8192 + (vr ^ xr) * 256 + n * 16);
    }
#define LDK(i) asm volatile("global_load_dwordx4 %0, %1, %2 nt" : "=v"(Kr[i]) : "v"(koffs[i]), "s"(kc) : "memory")
#define VOFS [o0] "v"(voffs[0]), [o1] "v"(voffs[1]), [o2] "v"(voffs[2]), [o3] "v"(voffs[3]), [o4] "v"(voffs[4]), [o5] "v"(voffs[5]), [o6] "v"(voffs[6]), [o7] "v"(voffs[7])
#define LDV(c) "global_load_dwordx4 %[v" #c "], %[o" #c "], %[base]" VNT "\n\t"
    PIN();
    NOW(1);
    u32x4 Kr[8], Vr[8];
    Kr[0] = Wt[0]; Kr[1] = Wt[1]; Kr[2] = Wt[2]; Kr[3] = Wt[3];
    if (STO & 4) asm volatile("s_load_dword %0, %1, %2" : "=s"(to2) : "s"(vc), "s"(tkv) : "memory");
    if (STO & 8) {
      const char* tp = (const char*)wo + (u32)((kvo * 512 + 64 * ab) * DM) * 2u;
#define TSEL(k, p) asm("s_cmp_eq_u32 %2, " #k "\n\ts_cselect_b64 %0, %1, %0" : "+s"(tp) : "s"((const char*)(p)), "s"(sw) : "scc")
      TSEL(1, (const char*)out);
      TSEL(2, (const char*)w2 + (u32)(64 * (r < 28 ? r : 0) * DM) * 2u);
      TSEL(3, ws + YP_OFF);
      TSEL(4, (const char*)w1);
      TSEL(5, (const char*)w1 + (u32)((DM - 1) * FF) * 2u);
      TSEL(6, b1);
      TSEL(7, b2);
      asm volatile("s_load_dword %0, %1, 0" : "=s"(to3) : "s"(tp) : "memory");
    }
    __builtin_amdgcn_sched_barrier(0);
    u32 opub = (lane & 3) ? OOB : OP_OFF + (u32)((((kvh * 8 + sw) * 8 + ab) * 4 + aj) * 256 + lane * 4);
    u32 spub = g >= 2 ? OOB : SP_OFF + (u32)(((128 * oj + 16 * sw + n) >> 2) * 8192 + (kvo * 8 + ab) * 128 + g * 64 + (n & 3) * 16);
    u32 ooff = OP_OFF + (u32)(((kvo * 8 + ab) * 8 + sw) * 1024 + lane * 16);
    u32 soff = OS_OFF + (u32)(((kvo * 8 + sw) * 4 + g) * 128 + (ab >> 1) * 8);
    auto swz = [](int h) { return (h & 1) | ((h >> 1) << 2); };
    u32 slw = lds_addr(&s_o[sw * 512 + ((n & 3) * 2 + (g >> 1)) * 64 + 4 * (((g & 1) * 8 + 2 * (n >> 2)) ^ swz(n & 3))]), slw2 = slw ^ 16u;
    u32 slr = lds_addr(&s_o[(lane >> 5) * 2048 + sw * 64 + 4 * (((lane & 31) >> 1) ^ swz(sw >> 1)) + 2 * (lane & 1)]);
    u32 oaw = lds_addr(&s_oA[sw * OA_LD + 32 * ((n >> 2) & 1) + 16 * (g & 1) + 8 * (n >> 3) + 2 * (n & 3) + (g >> 1)]);
    u32 oar = lds_addr(&s_oA[(n & 7) * OA_LD + 8 * g]);
    u32 wor = lds_addr(s_wo + sw * 32 + (8 * g + (n >> 2)) * S128 + (n & 3) * 8);
    const u32 qia = lds_addr(&s_q[(n & 3) * Q_LD + ((n >> 2) & 1) * 32 + 8 * g]), zla = lds_addr(&s_flag[24]);
    u32 qa0 = n < 8 ? qia : zla, qa1 = n < 8 ? zla : qia;
    u32 mla = lds_addr(&s_ml[n < 4 ? ((n * 8 + sw) * 4 + g) * 2 : 256 + 2 * lane]);
    u32 mxr = lds_addr(&s_flag[60 + (lane & 3)]), mxa = lane < 4 ? mxr : lds_addr(&s_ml[256 + lane]);
    asm volatile("" : "+v"(opub), "+v"(spub), "+v"(ooff), "+v"(soff), "+v"(slw), "+v"(slr), "+v"(oaw), "+v"(oar), "+v"(wor), "+v"(qa0), "+v"(qa1),
                 "+v"(mla), "+v"(mxr), "+v"(mxa), "+v"(slw2));
    u32 fa = fme, c2 = 2u, c4 = 4u, c6 = 6u;
    asm volatile("" : "+v"(fa), "+v"(c2), "+v"(c4), "+v"(c6));
    u32x4 Pm[4] = {};
    asm volatile("" : "+v"(Pm[0]), "+v"(Pm[1]), "+v"(Pm[2]), "+v"(Pm[3]));
#pragma unroll
    for (int i = NB0; i < 8; ++i) {
      if (WTB[i] < 16) asm volatile("s_waitcnt vmcnt(%0)" ::"n"(WTB[i] & 15) : "memory");
      LDK(i);
    }
    asm volatile("" ::"v"(cpf));
    NOW(2);
    static_assert(k_safe(), "all of K has landed at vmcnt(8)");
    u32 st, tmp, tm, el;
#define CKA(c)                                                                                                   \
  ".if " #c " < %[qb]\n\ts_waitcnt vmcnt(%[w" #c "])\n\t.else\n\t"                                               \
  "s_mov_b32 %[st], %[l" #c "]\n.Lnb" #c "_%=:\n\t.if %[gd]\n\ts_memrealtime vcc\n\t.endif\n\t"                  \
  "ds_read_b32 %[t], %[fq]\n\ts_waitcnt lgkmcnt(0)\n\t.if %[gd]\n\ts_sub_u32 %[el], vcc_lo, %[tz]\n\t.endif\n\t" \
  "v_cmp_lt_u32 vcc, 1, %[t]\n\ts_getreg_b32 %[tm], hwreg(HW_REG_IB_STS, 0, 4)\n\ts_cmp_eq_u64 vcc, exec\n\t"    \
  ".if %[l" #c "] > " #c "\n\ts_cbranch_scc1 .Lb" #c "_%=\n\t.else\n\ts_cbranch_scc1 .Lqe%=\n\t.endif\n\t"       \
  ".if %[gd]\n\ts_cmp_ge_u32 %[el], %[g0] + " #c " * %[gd]\n\ts_cselect_b32 %[tm], 0, %[tm]\n\t.endif\n\t"      \
  "s_cmp_gt_u32 %[tm], %[w" #c "]\n\ts_cbranch_scc1 .Lnb" #c "_%=\n\t.endif\n\t" LDV(c)
#define CKE(c, c1) ".if " #c " >= %[qb]\n.Lb" #c "_%=:\n\t" LDV(c) ".if " #c " >= 3 && " #c " < 7\n\ts_cmp_eq_u32 %[st], " #c1 "\n\ts_cbranch_scc1 .Lqe%=\n\t.endif\n\t.endif\n\t"
    asm volatile(CKA(0) CKA(1) CKA(2) CKA(3) CKA(4) CKA(5) CKA(6) CKA(7)
                 "s_mov_b32 %[st], 8\n\tds_write_b32 %[fme], %[one] offset:32\n"
                 ".Lqw%=:\n\tds_read_b32 %[t], %[fq]\n\ts_waitcnt lgkmcnt(0)\n\tv_cmp_lt_u32 vcc, 1, %[t]\n\ts_cmp_eq_u64 vcc, exec\n\t"
                 "s_cbranch_scc1 .Lqe%=\n\ts_sleep 1\n\ts_branch .Lqw%=\n\t"
                 CKE(0, 1) CKE(1, 2) CKE(2, 3) CKE(3, 4) CKE(4, 5) CKE(5, 6) CKE(6, 7) CKE(7, 8) "\n.Lqe%=:"
                 : [v0] "=&v"(Vr[0]), [v1] "=&v"(Vr[1]), [v2] "=&v"(Vr[2]), [v3] "=&v"(Vr[3]), [v4] "=&v"(Vr[4]), [v5] "=&v"(Vr[5]), [v6] "=&v"(Vr[6]),
                   [v7] "=&v"(Vr[7]), [st] "=&s"(st), [t] "=&v"(tmp), [tm] "=&s"(tm), [el] "=&s"(el)
                 : VOFS, [base] "s"(vc), [fq] "v"(fQ), [fme] "v"(fa), [one] "v"(c2), [qb] "n"(QB0), [gd] "n"(GD), [g0] "n"(GT0), [tz] "s"(tg0),
                   [w0] "n"(WTB[8]), [w1] "n"(WTB[9]), [w2] "n"(WTB[10]), [w3] "n"(WTB[11]), [w4] "n"(WTB[12]), [w5] "n"(WTB[13]), [w6] "n"(WTB[14]), [w7] "n"(WTB[15]),
                   [l0] "n"(lim_of(0)), [l1] "n"(lim_of(1)), [l2] "n"(lim_of(2)), [l3] "n"(lim_of(3)), [l4] "n"(lim_of(4)), [l5] "n"(lim_of(5)), [l6] "n"(lim_of(6)),
                   [l7] "n"(lim_of(7))
                 : "vcc", "scc", "memory");
    asm volatile("" ::"s"(to2), "s"(to3));
    YNOW(4);
    float S0, S1, mw;
    u32 tagv;
    {
      u32x4 B0 = *(const LAS u32x4*)(uintptr_t)qa0, B1 = *(const LAS u32x4*)(uintptr_t)(qa0 + 128u);
      u32x4 B2 = *(const LAS u32x4*)(uintptr_t)qa1, B3 = *(const LAS u32x4*)(uintptr_t)(qa1 + 128u);
      tagv = s_tag;
      asm volatile("s_waitcnt vmcnt(8)" : "+v"(Kr[4]), "+v"(Kr[5]), "+v"(Kr[6]), "+v"(Kr[7]), "+v"(B0)::"memory");
      f32x4 D0 = mfma(Kr[0], B0, f32x4{0.f, 0.f, 0.f, 0.f}), D1 = mfma(Kr[4], B0, f32x4{0.f, 0.f, 0.f, 0.f});
      D0 = mfma(Kr[1], B1, D0); D1 = mfma(Kr[5], B1, D1);
      D0 = mfma(Kr[2], B2, D0); D1 = mfma(Kr[6], B2, D1);
      D0 = mfma(Kr[3], B3, D0); D1 = mfma(Kr[7], B3, D1);
      float X0 = D0[0] + dpp<0x104>(D0[1]);
      asm("v_add_f32_dpp %0, %1, %2 row_shr:4 row_mask:0xf bank_mask:0xa" : "+v"(X0) : "v"(D0[2]), "v"(D0[3]));
      float X1 = D1[0] + dpp<0x104>(D1[1]);
      asm("v_add_f32_dpp %0, %1, %2 row_shr:4 row_mask:0xf bank_mask:0xa" : "+v"(X1) : "v"(D1[2]), "v"(D1[3]));
      const u32 R = pack_bf(X0, X1);
      const f32x2 Y = f32x2{bf_lo(R), bf_hi(R)} * 0.08838834764831845f;
      const u32 R2 = pack_bf(Y[0], Y[1]);
      S0 = bf_lo(R2); S1 = bf_hi(R2);
      float mt;
      asm volatile("v_max_f32 %0, %2, %3\n\ts_nop 1\n\t"
                   "v_max_f32_dpp %0, %0, %0 row_ror:4 row_mask:0xf bank_mask:0xf\n\ts_nop 1\n\t"
                   "v_max_f32_dpp %1, %0, %0 row_ror:8 row_mask:0xf bank_mask:0xf\n\t"
                   "v_max_f32_dpp %0, %0, %0 row_ror:8 row_mask:0xf bank_mask:0xf\n\ts_nop 1\n\t"
                   "v_permlane16_swap_b32 %0, %1\n\t"
                   "v_max_f32 %0, %0, %1\n\tv_mov_b32 %1, %0\n\ts_nop 1\n\t"
                   "v_permlane32_swap_b32 %0, %1\n\t"
                   "v_max_f32 %0, %0, %1\n\t"
                   "ds_max_f32 %4, %0"
                   : "=&v"(mw), "=&v"(mt) : "v"(S0), "v"(S1), "v"(mxa) : "memory");
    }
    {
      const f32x2 Y = (f32x2{S0, S1} - mw) * 1.4426950408889634f;
      const float p0 = __builtin_amdgcn_exp2f(Y[0]), p1 = __builtin_amdgcn_exp2f(Y[1]);
      const int P = (int)pack_bf(p0, p1);
#define PMV(b, d) Pm[b][d] = (u32)__builtin_amdgcn_update_dpp((int)Pm[b][d], P, (d) == (b) ? 0xE4 : (d) > (b) ? 0x110 + 4 * ((d) - (b)) : 0x100 + 4 * ((b) - (d)), 0xF, 1 << (d), false)
#define PMB(b) PMV(b, 0); PMV(b, 1); PMV(b, 2); PMV(b, 3)
      PMB(0); PMB(1); PMB(2); PMB(3);
      float l = p0 + p1;
      l += dpp<0x124>(l); l += dpp<0x128>(l);
      *(LAS f32x2*)(uintptr_t)mla = f32x2{l, mw};
      flag_set(fa, c2);
    }
    YNOW(5);
#define CKB(c) ".Lv" #c "_%=:\n\t.if %[w" #c "] < 8\n\ts_waitcnt vmcnt(%[w" #c "])\n\t.endif\n\t" LDV(c)
#define CKJ(c) "s_cmp_eq_u32 %[st], " #c "\n\ts_cbranch_scc1 .Lv" #c "_%=\n\t"
    asm volatile(CKJ(8) CKJ(7) CKJ(6) CKJ(5) ".if %[l3] == 3\n\t" CKJ(4) CKB(3) ".endif\n\t" CKB(4) CKB(5) CKB(6) CKB(7) "\n.Lv8_%=:\n\tds_write_b32 %[fme], %[one] offset:32"
                 : [v3] "+v"(Vr[3]), [v4] "+v"(Vr[4]), [v5] "+v"(Vr[5]), [v6] "+v"(Vr[6]), [v7] "+v"(Vr[7]), "+v"(Pm[0]), "+v"(Pm[1]), "+v"(Pm[2]), "+v"(Pm[3])
                 : VOFS, [base] "s"(vc), [st] "s"(st), [fme] "v"(fa), [one] "v"(c2), [l3] "n"(lim_of(3)), [w3] "n"(WTC[0]), [w4] "n"(WTC[1]), [w5] "n"(WTC[2]), [w6] "n"(WTC[3]), [w7] "n"(WTC[4])
                 : "scc", "memory");
    YNOW(3);
    asm volatile("" : "+v"(tagv));
    const u32 tag = __builtin_amdgcn_readfirstlane(tagv);
    const u32 t0 = tag & 0x7FFu, t1 = (tag >> 11) & 0x7FFu;
    u32 tl = (lane & 1) ? t1 : t0, tv0 = t0, tv1 = t1;
    asm volatile("" : "+v"(tl), "+v"(tv0), "+v"(tv1));
    __builtin_amdgcn_sched_barrier(0);
    {
      f32x4 D0 = {0.f, 0.f, 0.f, 0.f}, D1 = D0;
      float f;
#pragma unroll
      for (int b = 0; b < 5; ++b) {
        if (b == MPOS) {
          float m;
          u32 fl;
          asm volatile(".Lmw%=:\n\tds_read_b32 %0, %2\n\tds_read_b32 %1, %3\n\ts_waitcnt lgkmcnt(0)\n\tv_cmp_gt_u32 vcc, 2, %0\n\ts_cbranch_vccnz .Lmw%="
                       : "=&v"(fl), "=&v"(m) : "v"(fS), "v"(mxr) : "vcc", "memory");
          f = __expf(mw - m);
          asm volatile("" : "+v"(f));
          YNOW(6);
          __builtin_amdgcn_sched_barrier(0);
        }
        if (b == 4) break;
        asm volatile("s_waitcnt vmcnt(%2)" : "+v"(Vr[2 * b]), "+v"(Vr[2 * b + 1]) : "n"(6 - 2 * b) : "memory");
        if (b == 3) NOW(7);
        if (WOP == 3 && b == WOB) flag_set(fa, 3u);
        u32x4 T0, T1;
#pragma unroll
        for (int v = 0; v < 4; ++v) T0[v] = __builtin_amdgcn_perm(Vr[2 * b + 1][v], Vr[2 * b][v], 0x05040100u);
        D0 = mfma(T0, Pm[b], D0);
#pragma unroll
        for (int v = 0; v < 4; ++v) T1[v] = __builtin_amdgcn_perm(Vr[2 * b + 1][v], Vr[2 * b][v], 0x07060302u);
        D1 = mfma(T1, Pm[b], D1);
        __builtin_amdgcn_sched_barrier(0);
      }
      *(LAS f32x4*)(uintptr_t)slw = D0 * f;
      *(LAS f32x4*)(uintptr_t)slw2 = D1 * f;
    }
    flag_set(fa, c4);
    {
      f32x2 s0, s1, s2, s3;
      u32 fl;
      if (MRG & 1)
        asm volatile(".Lsw%=:\n\tds_read_b32 %0, %5\n\tds_read_b64 %1, %6\n\tds_read_b64 %2, %6 offset:2048\n\tds_read_b64 %3, %6 offset:4096\n\t"
                     "ds_read_b64 %4, %6 offset:6144\n\ts_waitcnt lgkmcnt(4)\n\tv_cmp_gt_u32 vcc, 4, %0\n\ts_waitcnt lgkmcnt(0)\n\ts_cbranch_vccnz .Lsw%="
                     : "=&v"(fl), "=&v"(s0), "=&v"(s1), "=&v"(s2), "=&v"(s3) : "v"(fS), "v"(slr) : "vcc", "memory");
      else if (MRG & 16)
        asm volatile("ds_read_b32 %0, %5\n\tds_read_b64 %1, %6\n\tds_read_b64 %2, %6 offset:2048\n\tds_read_b64 %3, %6 offset:4096\n\t"
                     "ds_read_b64 %4, %6 offset:6144\n\ts_waitcnt lgkmcnt(4)\n\tv_cmp_gt_u32 vcc, 4, %0\n\ts_cbranch_vccz .Lse%=\n"
                     ".Lsw%=:\n\tds_read_b32 %0, %5\n\ts_waitcnt lgkmcnt(0)\n\tv_cmp_gt_u32 vcc, 4, %0\n\ts_cbranch_vccnz .Lsw%=\n\t"
                     "ds_read_b64 %1, %6\n\tds_read_b64 %2, %6 offset:2048\n\tds_read_b64 %3, %6 offset:4096\n\tds_read_b64 %4, %6 offset:6144\n"
                     ".Lse%=:\n\ts_waitcnt lgkmcnt(0)"
                     : "=&v"(fl), "=&v"(s0), "=&v"(s1), "=&v"(s2), "=&v"(s3) : "v"(fS), "v"(slr) : "vcc", "memory");
      else
        asm volatile(".Lsw%=:\n\tds_read_b32 %0, %5\n\ts_waitcnt lgkmcnt(0)\n\tv_cmp_gt_u32 vcc, 4, %0\n\ts_cbranch_vccnz .Lsw%=\n\t"
                     "ds_read_b64 %1, %6\n\tds_read_b64 %2, %6 offset:2048\n\tds_read_b64 %3, %6 offset:4096\n\tds_read_b64 %4, %6 offset:6144\n\t"
                     "s_waitcnt lgkmcnt(0)"
                     : "=&v"(fl), "=&v"(s0), "=&v"(s1), "=&v"(s2), "=&v"(s3) : "v"(fS), "v"(slr) : "vcc", "memory");
      NOW(8);
      const f32x2 a2 = (s0 + s1) + (s2 + s3);
      auto p = __builtin_amdgcn_permlane32_swap(asu(a2[0]), asu(a2[1]), false, false);
      const u32 e = enc(asf(p[0]) + asf(p[1]), tl);
      const u32x4 e4 = {e, (u32)__builtin_amdgcn_update_dpp(0, (int)e, 0x55, 0xF, 0xF, true), (u32)__builtin_amdgcn_update_dpp(0, (int)e, 0xAA, 0xF, 0xF, true),
                        (u32)__builtin_amdgcn_update_dpp(0, (int)e, 0xFF, 0xF, 0xF, true)};
      ST_SC(e4, rs_w, opub);
    }
    PIN();
    u32x2 sv;
    u32x4 ov;
    if (STE) LOADW(2, sv, soff, 0);
    if (WOP == 5) flag_set(fa, 5u);
    if (OACK == 1) WAITV(0);
    if (OACK > 1) __builtin_amdgcn_s_sleep(OACK);
    if (!STE) LOADW(2, sv, soff, 0);
    LOADW(4, ov, ooff, 0);
    NOW(9);
    u32x4 A0, A1;
    if (!WOL) {
      FWAIT(0, fQ, 3u);
      A0 = tr_read_a(wor, S128); A1 = tr_read_a(wor + 32 * S128, S128);
      asm volatile("" : "+v"(A0), "+v"(A1)::"memory");
    }
    asm volatile("s_waitcnt vmcnt(1)" : "+v"(sv)::"memory");
    if (__builtin_expect(lanes_set(or_and(sv[0] ^ t0, sv[1] ^ t1, 0x7FFu)) != 0ull, 0)) {
#pragma nounroll
      for (int spin = 0; spin < SPIN_CAP; ++spin) {
        asm volatile("s_waitcnt vmcnt(0)" : "+v"(ov), "+v"(sv)::"memory");
        const unsigned long long bad = lanes_set(or_and(sv[0] ^ t0, sv[1] ^ t1, 0x7FFu));
        if (bad == 0ull) break;
        RELOAD(2, sv, bad, soff, 0);
        RELOAD(4, ov, lanes_set(tdiff(ov, t0, t1) & 0x7FFu), ooff, 0);
      }
    }
    float wj;
    {
      const float m = dec(sv[0]);
      wj = __expf(m - xor32_max(xor16_max(m)));
      wj *= __builtin_amdgcn_rcpf(xor32_add(xor16_add(wj * dec(sv[1]))));
    }
    asm volatile("" : "+v"(wj));
    {
      float z0, z1;
      unsigned long long bad = ONP > 1 ? ~0ull : 0ull;
      int spin = SPIN_CAP;
#pragma unroll
      for (int k = 1; k < ONP; ++k) {
        if (k > 1) __builtin_amdgcn_s_sleep(OPD); else if (OPD1) __builtin_amdgcn_s_sleep(OPD1);
        if (k + 1 < ONP) LOADA(4, ov, ooff, 0);
      }
#pragma nounroll
      do {
        RELOAD(4, ov, bad, ooff, 0);
        u32 msk;
        asm volatile(CAPN("0x7ff", STR(ONP - 1)) : "+s"(spin), "=s"(msk), "+v"(ov)::"scc", "memory");
        u32x4 u = untag(ov, t0, t1);
        asm volatile("" : "+v"(u));
        bad = lanes_set(or_and(u[0] | u[1] | u[2], u[3], msk));
        asm volatile("" : "+s"(bad));
        const f32x4 y = __builtin_bit_cast(f32x4, u) * wj;
        auto p = __builtin_amdgcn_permlane32_swap(asu(y[0]), asu(y[2]), false, false);
        auto q = __builtin_amdgcn_permlane32_swap(asu(y[1]), asu(y[3]), false, false);
        z0 = asf(p[0]) + asf(p[1]); z1 = asf(q[0]) + asf(q[1]);
        asm volatile("" : "+v"(z0), "+v"(z1));
      } while (bad != 0ull);
      auto p = __builtin_amdgcn_permlane16_swap(asu(z0), asu(z1), false, false);
      const float o = asf(p[0]) + asf(p[1]);
      *(LAS u16*)(uintptr_t)oaw = (u16)pack_bf(o, o);
    }
    flag_set(fa, c6);
    NOW(10);
    if (WOL) {
      FWAIT(0, fQ, 3u);
      A0 = tr_read_a(wor, S128); A1 = tr_read_a(wor + 32 * S128, S128);
      PIN();
    }
    {
      u32x4 a0, a1;
      u32 fl;
      if (MRG & 2)
        asm volatile(".Low%=:\n\tds_read_b32 %0, %3\n\tds_read_b128 %1, %4\n\tds_read_b128 %2, %4 offset:64\n\t"
                     "s_waitcnt lgkmcnt(2)\n\tv_cmp_gt_u32 vcc, 6, %0\n\ts_waitcnt lgkmcnt(0)\n\ts_cbranch_vccnz .Low%="
                     : "=&v"(fl), "=&v"(a0), "=&v"(a1) : "v"(fS), "v"(oar) : "vcc", "memory");
      else if (MRG & 32)
        asm volatile("ds_read_b32 %0, %3\n\tds_read_b128 %1, %4\n\tds_read_b128 %2, %4 offset:64\n\t"
                     "s_waitcnt lgkmcnt(2)\n\tv_cmp_gt_u32 vcc, 6, %0\n\ts_cbranch_vccz .Loe%=\n"
                     ".Low%=:\n\tds_read_b32 %0, %3\n\ts_waitcnt lgkmcnt(0)\n\tv_cmp_gt_u32 vcc, 6, %0\n\ts_cbranch_vccnz .Low%=\n\t"
                     "ds_read_b128 %1, %4\n\tds_read_b128 %2, %4 offset:64\n.Loe%=:\n\ts_waitcnt lgkmcnt(0)"
                     : "=&v"(fl), "=&v"(a0), "=&v"(a1) : "v"(fS), "v"(oar) : "vcc", "memory");
      else
        asm volatile(".Low%=:\n\tds_read_b32 %0, %3\n\ts_waitcnt lgkmcnt(0)\n\tv_cmp_gt_u32 vcc, 6, %0\n\ts_cbranch_vccnz .Low%=\n\t"
                     "ds_read_b128 %1, %4\n\tds_read_b128 %2, %4 offset:64\n\ts_waitcnt lgkmcnt(0)"
                     : "=&v"(fl), "=&v"(a0), "=&v"(a1) : "v"(fS), "v"(oar) : "vcc", "memory");
      NOW(11);
      f32x4 D = mfma(a0, A0, f32x4{0.f, 0.f, 0.f, 0.f});
      D = mfma(a1, A1, D);
      __builtin_amdgcn_raw_buffer_store_b128(enc4(D, tv0, tv1), rs_w, spub, 0, STAUX);
    }
    if (SACK) WAITV(0);
    if (ONP > 1) asm volatile("" ::"v"(ov));
    flag_set(fa, 7u);
    __builtin_amdgcn_endpgm();
  }
}

}

extern "C" void kernel_launch(void* const* d_in, const int* in_sizes, int n_in, void* d_out, int out_size,
                              void* d_ws, size_t ws_size, hipStream_t stream) {
  fused_kernel<<<256, 768, 0, stream>>>((const u16*)d_in[0], (const u16*)d_in[3], (const u16*)d_in[6], (char*)d_ws,
                                        (const u16*)d_in[1], (const u16*)d_in[2], (const u16*)d_in[4],
                                        (const u16*)d_in[8], (const u16*)d_in[7], (const u16*)d_in[9], (u16*)d_out);
}
```
